```python
import torch

def _softmax(x, axis=-1):
    return torch.softmax(x, dim=axis)
def _gelu(x):
    return torch.nn.functional.gelu(x, approximate="tanh")
def _silu(x):
    return torch.nn.functional.silu(x)
def _sigmoid(x):
    return torch.sigmoid(x)
def _layernorm(x, axis=-1, eps=1e-5):
    m = x.mean(dim=axis, keepdim=True)
    v = x.var(dim=axis, keepdim=True, unbiased=False)
    return (x - m) / torch.sqrt(v + eps)
def _scatter_add(tgt, idx, src, axis=0):
    return tgt.clone().index_add_(axis, idx, src)
def _segment_sum(src, seg, n):
    out = torch.zeros((n,) + src.shape[1:], dtype=src.dtype)
    return out.index_add_(0, seg, src)
def _pad(x, pw):
    flat = tuple(v for pair in reversed(pw) for v in pair)
    return torch.nn.functional.pad(x, flat)

kernel_name = 'gated_mix__softmax_full_mha__softmax_full_mha__prompt'

def setup_inputs():

    _w = (lambda a: (lambda t: t.to(torch.bfloat16) if t.is_floating_point() else t)(torch.as_tensor(a)))
    def _A_setup():
        B, S, D = 1, 2048, 448
        return {
            "x": _w((lambda *s: torch.randn(*s, dtype=torch.bfloat16) * 0.05)(B, S, D)),
            "wqkv": _w((lambda *s: torch.randn(*s, dtype=torch.bfloat16) * 0.8)(D, 3 * D)),
            "wo": _w((lambda *s: torch.randn(*s, dtype=torch.bfloat16) * 0.05)(D, D)),
        }
    _A_in = _A_setup()

    def _B_setup():
        B, S, D = 1, 2048, 448
        return {
            "x": _w((lambda *s: torch.randn(*s, dtype=torch.bfloat16) * 0.05)(B, S, D)),
            "wqkv": _w((lambda *s: torch.randn(*s, dtype=torch.bfloat16) * 0.8)(D, 3 * D)),
            "wo": _w((lambda *s: torch.randn(*s, dtype=torch.bfloat16) * 0.05)(D, D)),
        }
    _B_in = _B_setup()

    return {
        "A_x": _A_in["x"],
        "A_wqkv": _A_in["wqkv"],
        "A_wo": _A_in["wo"],
        "B_x": _B_in["x"],
        "B_wqkv": _B_in["wqkv"],
        "B_wo": _B_in["wo"],
        "gate_w": _w((lambda *s: torch.randn(*s, dtype=torch.bfloat16) * 0.05)(448) * 16.0),
    }

def reference(A_x, A_wqkv, A_wo, B_x, B_wqkv, B_wo, gate_w):
    def _A_ref(x, wqkv, wo):
        B, S, D = x.shape
        H = 16; Dh = D // H
        qkv = (lambda x, *d: x.permute(*d))((x @ wqkv).reshape(B, S, 3, H, Dh), 2, 0, 3, 1, 4)
        q, k, v = qkv[0], qkv[1], qkv[2]
        scale = Dh ** -0.5
        scores = torch.einsum("bhqd,bhkd->bhqk", q, k) * scale
        p = _softmax(scores, axis=-1)
        o = torch.einsum("bhqk,bhkd->bhqd", p, v)
        return (lambda x, *d: x.permute(*d))(o, 0, 2, 1, 3).reshape(B, S, D) @ wo

    def _B_ref(x, wqkv, wo):
        B, S, D = x.shape
        H = 16; Dh = D // H
        qkv = (lambda x, *d: x.permute(*d))((x @ wqkv).reshape(B, S, 3, H, Dh), 2, 0, 3, 1, 4)
        q, k, v = qkv[0], qkv[1], qkv[2]
        scale = Dh ** -0.5
        scores = torch.einsum("bhqd,bhkd->bhqk", q, k) * scale
        p = _softmax(scores, axis=-1)
        o = torch.einsum("bhqk,bhkd->bhqd", p, v)
        return (lambda x, *d: x.permute(*d))(o, 0, 2, 1, 3).reshape(B, S, D) @ wo

    x = A_x
    g = _sigmoid((x * gate_w).sum(-1, keepdims=True))
    a = _A_ref(x, A_wqkv, A_wo)
    b = _B_ref(x, B_wqkv, B_wo)
    return g * a + (1 - g) * b
```

```cpp
#include <hip/hip_runtime.h>
#include <hip/hip_bf16.h>
#include <stdint.h>

typedef short    bf16x8 __attribute__((ext_vector_type(8)));
typedef short    s16x4  __attribute__((ext_vector_type(4)));
typedef float    f32x16 __attribute__((ext_vector_type(16)));
typedef float    f32x2  __attribute__((ext_vector_type(2)));
typedef float    f32x4  __attribute__((ext_vector_type(4)));
typedef __bf16   bf16x2 __attribute__((ext_vector_type(2)));
typedef unsigned u32x4  __attribute__((ext_vector_type(4)));
typedef unsigned u32x2  __attribute__((ext_vector_type(2)));
typedef __amdgpu_buffer_rsrc_t rsrc_t;
#define DI __device__ __forceinline__
#define LAS __attribute__((address_space(3)))
#define GAS __attribute__((address_space(1)))
typedef LAS char* lptr;
typedef GAS unsigned long long gu64;

#define KVSC "sc1"
#define WOAUX 16
#define XBYTES (SEQ * XROW)
#define WBYTES (DM * WROW)
#define SETPRIO(p) __builtin_amdgcn_s_setprio(p)
#define MFMA(a, b, c) __builtin_amdgcn_mfma_f32_32x32x16_bf16((a), (b), (c), 0, 0, 0)
#define SBAR() __builtin_amdgcn_sched_barrier(0)
#define PIN(x) asm volatile("" : "+v"(x))
#define WAIT_BAR(N) asm volatile("s_waitcnt vmcnt(" #N ") lgkmcnt(0)\n\ts_barrier" ::: "memory")
#define RLX_AGENT __ATOMIC_RELAXED, __HIP_MEMORY_SCOPE_AGENT

constexpr int SEQ = 2048, DM = 448, XROW = DM * 2, WROW = 3 * DM * 2;
constexpr unsigned WS_FLAG = 4096, WS_KV = 1u << 20, WS_WOT = WS_KV + (8u << 20), WS_OP = WS_WOT + (4u << 20), WS_END = WS_OP + (4u << 20);
constexpr unsigned WS_DBG = WS_END;
constexpr int SLOT = 32768, LDS_MISC = 4 * SLOT, LDS_GW = LDS_MISC + 16384, LDS_G = LDS_GW + 1024, LDS_BYTES = LDS_G + 256;
constexpr unsigned SPIN_CAP = 1u << 18;
constexpr float QSCALE = 0.18898223650461362f * 1.4426950408889634f;

DI unsigned cvtpk(float lo, float hi) { f32x2 v = {lo, hi}; return __builtin_bit_cast(unsigned, __builtin_convertvector(v, bf16x2)); }
DI bf16x8 pack8(const f32x16& x, int s) {
  u32x4 p = {cvtpk(x[8 * s], x[8 * s + 1]), cvtpk(x[8 * s + 2], x[8 * s + 3]), cvtpk(x[8 * s + 4], x[8 * s + 5]), cvtpk(x[8 * s + 6], x[8 * s + 7])};
  return __builtin_bit_cast(bf16x8, p);
}
DI void dma16(unsigned lds_dst, unsigned voff, rsrc_t rs, unsigned soff) {
  asm volatile("s_mov_b32 m0, %0\n\ts_nop 0\n\tbuffer_load_dwordx4 %1, %2, %3 offen lds" ::"s"(lds_dst), "v"(voff), "s"(rs), "s"(soff) : "memory");
}
DI void dma16_dev(unsigned lds_dst, unsigned voff, rsrc_t rs, unsigned soff) {
  asm volatile("s_mov_b32 m0, %0\n\ts_nop 0\n\tbuffer_load_dwordx4 %1, %2, %3 offen " KVSC " lds" ::"s"(lds_dst), "v"(voff), "s"(rs), "s"(soff) : "memory");
}
DI bf16x8 ldsr(lptr p) { return *(const LAS bf16x8*)p; }
DI s16x4 trr(lptr p) { return __builtin_amdgcn_ds_read_tr16_b64_v4i16((LAS s16x4*)p); }
DI bf16x8 trr2(lptr p, int second) { s16x4 lo = trr(p), hi = trr(p + second); return __builtin_shufflevector(lo, hi, 0, 1, 2, 3, 4, 5, 6, 7); }
DI float swap_max(float m) { auto r = __builtin_amdgcn_permlane32_swap(__float_as_uint(m), __float_as_uint(m), false, false); return fmaxf(__uint_as_float(r[0]), __uint_as_float(r[1])); }
DI float rowmax16(const f32x16& s) {
  float a = fmaxf(fmaxf(s[0], s[1]), s[2]), b = fmaxf(fmaxf(s[3], s[4]), s[5]);
  a = fmaxf(fmaxf(a, s[6]), s[7]); b = fmaxf(fmaxf(b, s[8]), s[9]); a = fmaxf(fmaxf(a, s[10]), s[11]); b = fmaxf(fmaxf(b, s[12]), s[13]);
  a = fmaxf(fmaxf(a, s[14]), s[15]);
  return swap_max(fmaxf(a, b));
}
DI float dot2(unsigned a, unsigned b, float c) { return __builtin_amdgcn_fdot2_f32_bf16(__builtin_bit_cast(bf16x2, a), __builtin_bit_cast(bf16x2, b), c, false); }
DI float bflo(unsigned u) { return __uint_as_float(u << 16); }
DI float bfhi(unsigned u) { return __uint_as_float(u & 0xffff0000u); }

#define STAMP(i) do { } while (0)

__global__ __launch_bounds__(512, 2) void gated_mha_fused(const char* __restrict__ x, const char* __restrict__ wqkvA, const char* __restrict__ woA,
                                                          const char* __restrict__ wqkvB, const char* __restrict__ woB, const char* __restrict__ gate,
                                                          char* __restrict__ out, char* ws) {
  __shared__ __attribute__((aligned(1024))) char lds_raw[LDS_BYTES];
  STAMP(0);
  const lptr lds = (lptr)lds_raw;
  const unsigned lds0 = (unsigned)(uintptr_t)lds;
  const int tid = threadIdx.x, lane = tid & 63, r32 = lane & 31, hh = lane >> 5;
  const int w = __builtin_amdgcn_readfirstlane(tid >> 6), tt = w & 3, hs = w >> 2;
  const int bid = blockIdx.x, grp = (bid & 7) * 2 + ((bid >> 3) & 1), j = bid >> 4, br = grp >> 3, hp = grp & 7, tq = grp & 3, cq = grp >> 2;

  const rsrc_t rsX = __builtin_amdgcn_make_buffer_rsrc((void*)x, 0, XBYTES, 0x00020000);
  const rsrc_t rsW = __builtin_amdgcn_make_buffer_rsrc((void*)(br ? wqkvB : wqkvA), 0, WBYTES, 0x00020000);
  const rsrc_t rsWo = __builtin_amdgcn_make_buffer_rsrc((void*)((j >> 3) ? woB : woA), 0, DM * XROW, 0x00020000);
  const rsrc_t rsG = __builtin_amdgcn_make_buffer_rsrc((void*)gate, 0, XROW, 0x00020000);
  const rsrc_t rsWS = __builtin_amdgcn_make_buffer_rsrc((void*)ws, 0, WS_END, 0x00020000);

  const unsigned xrow = 16 * w + (lane >> 3);
  const unsigned xoff0 = (128 * j + xrow) * XROW + 16 * ((lane & 7) ^ ((lane >> 4) & 7));
  const unsigned xoff1 = (128 * j + xrow + 8) * XROW + 16 * ((lane & 7) ^ ((4 + (lane >> 4)) & 7));
  const unsigned wl = (lane >> 3) * WROW + (((lane >> 2) ^ (lane >> 4)) & 1) * 56 + 16 * (lane & 3) + hp * 112;
  const unsigned wkv_s = 16 * tt * WROW + (hs + 1) * XROW;
#define DMA_KV(c) do { const unsigned sl_ = lds0 + ((c) & 3) * SLOT; \
    dma16(sl_ + 2048 * w, xoff0, rsX, 128 * (c)); dma16(sl_ + 16384 + 2048 * w, wl, rsW, wkv_s + 64 * (c) * WROW); \
    dma16(sl_ + 2048 * w + 1024, xoff1, rsX, 128 * (c)); dma16(sl_ + 16384 + 2048 * w + 1024, wl, rsW, wkv_s + (64 * (c) + 8) * WROW); } while (0)
  DMA_KV(0);
  dma16(lds0 + LDS_GW, lane * 16, rsG, 0);
  DMA_KV(1); DMA_KV(2);
  const unsigned op_own = WS_OP + (((j * 4 + tt) * 2 + br) * 16 + 2 * hp + hs) * 2048;

  bf16x8 xf[28];
  f32x16 accK = {}, accV = {};
  float zg = 0.f;
  const unsigned xr = (32 * tt + r32) * 128 + ((hh ^ ((r32 >> 1) & 7)) * 16);
  const unsigned trl = (8 * hh + ((lane & 15) >> 2)) * 128 + ((hs ^ (lane >> 3)) & 1) * 64 + ((lane >> 4) & 1) * 32 + (lane & 3) * 8;
#define KV_CHUNK(c) do { const lptr s_ = lds + ((c) & 3) * SLOT; \
    _Pragma("unroll") for (int ks = 0; ks < 4; ++ks) { \
      xf[4 * (c) + ks] = ldsr(s_ + (xr ^ (32 * ks))); \
      const bf16x8 wk_ = trr2(s_ + 16384 + trl + ks * 2048, 512), wv_ = trr2(s_ + 16384 + 8192 + trl + ks * 2048, 512); \
      const u32x4 g_ = *(const LAS u32x4*)(lds + LDS_GW + hh * 16 + (4 * (c) + ks) * 32), x_ = __builtin_bit_cast(u32x4, xf[4 * (c) + ks]); \
      accK = MFMA(wk_, xf[4 * (c) + ks], accK); accV = MFMA(xf[4 * (c) + ks], wv_, accV); \
      _Pragma("unroll") for (int i = 0; i < 4; ++i) zg = dot2(x_[i], g_[i], zg); } } while (0)
  WAIT_BAR(8); STAMP(1); DMA_KV(3); KV_CHUNK(0);
  WAIT_BAR(8); DMA_KV(4); KV_CHUNK(1);
  WAIT_BAR(8); DMA_KV(5); KV_CHUNK(2);
  WAIT_BAR(8); DMA_KV(6); KV_CHUNK(3);
  WAIT_BAR(8);
  {
    const unsigned wol = (lane >> 2) * XROW + 16 * (lane & 3), wos = (56 * (j & 7) + 32 * (w & 1)) * XROW + 224 * cq + 64 * (w >> 1);
    dma16(lds0 + LDS_MISC + 2048 * w, wol, rsWo, wos); dma16(lds0 + LDS_MISC + 2048 * w + 1024, wol, rsWo, wos + 16 * XROW);
  }
#pragma unroll
  for (int q = 0; q < 4; ++q) dma16(lds0 + 3 * SLOT + 4096 * w + 1024 * q, wl, rsW, (32 * w + 8 * q) * WROW);
  KV_CHUNK(4);
  WAIT_BAR(10);
#pragma unroll
  for (int q = 0; q < 3; ++q) dma16(lds0 + 3072 * w + 1024 * q, wl, rsW, (256 + 24 * w + 8 * q) * WROW);
  { const u32x4 sent = {~0u, ~0u, ~0u, ~0u};
    __builtin_amdgcn_raw_buffer_store_b128(sent, rsWS, lane * 16, op_own, 16); __builtin_amdgcn_raw_buffer_store_b128(sent, rsWS, lane * 16 + 1024, op_own, 16); }
  unsigned long long epoch = __hip_atomic_load((gu64*)ws, RLX_AGENT);
  asm volatile("" ::: "memory");
  KV_CHUNK(5);
  WAIT_BAR(10); KV_CHUNK(6);
  STAMP(2);
  { auto r = __builtin_amdgcn_permlane32_swap(__float_as_uint(zg), __float_as_uint(zg), false, false); zg = __uint_as_float(r[0]) + __uint_as_float(r[1]);
    if (w == tq && lane < 32) *(LAS float*)(lds + LDS_G + lane * 4) = 1.0f / (1.0f + __expf(-zg)); }
  bf16x8 k0 = pack8(accK, 0), k1 = pack8(accK, 1), v0 = pack8(accV, 0), v1 = pack8(accV, 1);
  { u32x4 t = __builtin_bit_cast(u32x4, k1); t[2] = hh ? 0x00003f80u : t[2]; t[3] = hh ? 0u : t[3]; k1 = __builtin_bit_cast(bf16x8, t);
    u32x4 a = __builtin_bit_cast(u32x4, v0), b = __builtin_bit_cast(u32x4, v1); const unsigned pv = r32 == 28 ? 0x3f803f80u : 0u;
#pragma unroll
    for (int i = 0; i < 4; ++i) { a[i] = r32 >= 28 ? pv : a[i]; b[i] = r32 >= 28 ? pv : b[i]; }
    v0 = __builtin_bit_cast(bf16x8, a); v1 = __builtin_bit_cast(bf16x8, b); }
  { const unsigned kvo = WS_KV + (grp * 16 + j) * SLOT + w * 4096;
    __builtin_amdgcn_raw_buffer_store_b128(__builtin_bit_cast(u32x4, k0), rsWS, lane * 16, kvo, 16);
    __builtin_amdgcn_raw_buffer_store_b128(__builtin_bit_cast(u32x4, k1), rsWS, lane * 16 + 1024, kvo, 16);
    __builtin_amdgcn_raw_buffer_store_b128(__builtin_bit_cast(u32x4, v0), rsWS, lane * 16 + 2048, kvo, 16);
    __builtin_amdgcn_raw_buffer_store_b128(__builtin_bit_cast(u32x4, v1), rsWS, lane * 16 + 3072, kvo, 16);
    const lptr wp = lds + LDS_MISC + tt * 4096 + (28 * hs + 4 * hh + ((lane & 15) >> 2)) * 64 + ((lane >> 4) & 1) * 32 + (lane & 3) * 8;
    const bf16x8 t0 = trr2(wp, 512), t1 = trr2(wp + 1024, 512);
    const unsigned wto = WS_WOT + grp * 262144 + ((2 * j + hs) * 8 + tt) * 1024;
    __builtin_amdgcn_raw_buffer_store_b128(__builtin_bit_cast(u32x4, t0), rsWS, lane * 16, wto, 16);
    __builtin_amdgcn_raw_buffer_store_b128(__builtin_bit_cast(u32x4, t1), rsWS, lane * 16, wto + 4096, 16);
    const lptr own = lds + SLOT + w * 4096 + lane * 16;
    *(LAS bf16x8*)own = k0; *(LAS bf16x8*)(own + 1024) = k1; *(LAS bf16x8*)(own + 2048) = v0; *(LAS bf16x8*)(own + 3072) = v1; }
  f32x16 accQ = {};
  WAIT_BAR(12);
#pragma unroll
  for (int ks = 0; ks < 16; ++ks) accQ = MFMA(trr2(lds + 3 * SLOT + trl + ks * 2048, 512), xf[ks], accQ);
  WAIT_BAR(0);
  PIN(epoch); epoch += 1;
  if (tid == 0) __hip_atomic_store((gu64*)(ws + WS_FLAG + (grp * 16 + j) * 128), epoch, RLX_AGENT);
  STAMP(3);
#pragma unroll
  for (int ks = 0; ks < 12; ++ks) accQ = MFMA(trr2(lds + trl + ks * 2048, 512), xf[16 + ks], accQ);
#pragma unroll
  for (int i = 0; i < 16; ++i) accQ[i] *= QSCALE;
  bf16x8 q0 = pack8(accQ, 0), q1 = pack8(accQ, 1);
  { u32x4 t = __builtin_bit_cast(u32x4, q1); t[2] = hh ? 0u : t[2]; t[3] = hh ? 0u : t[3]; q1 = __builtin_bit_cast(bf16x8, t); }
  const bf16x8 q1z = q1;
  const f32x16 zero = {};
  {
    f32x16 s = MFMA(k0, q0, zero); s = MFMA(k1, q1, s);
    const float m = rowmax16(s);
    u32x4 t = __builtin_bit_cast(u32x4, q1); t[2] = hh ? (cvtpk(-m, 0.f) & 0xffffu) : t[2]; q1 = __builtin_bit_cast(bf16x8, t); }
  STAMP(4);

  { gu64* fp = (gu64*)(ws + WS_FLAG + (grp * 16 + (lane & 15)) * 128);
    for (unsigned spins = 0; spins < SPIN_CAP; ++spins) {
      const unsigned long long f = __hip_atomic_load(fp, RLX_AGENT);
      if (__all(f == epoch)) break;
      __builtin_amdgcn_s_sleep(2);
    }
    asm volatile("" ::: "memory"); }
  STAMP(5);
  const unsigned kvl = lane * 16, kvd = lds0 + w * 4096, kvs = WS_KV + grp * 16 * SLOT + w * 4096;
#define DMA_P(s, p) dma16_dev(kvd + (((s) + 1) & 3) * SLOT + (p) * 1024, kvl, rsWS, kvs + ((j + (s)) & 15) * SLOT + (p) * 1024)
  DMA_P(1, 0); DMA_P(1, 1); DMA_P(1, 2); DMA_P(1, 3); DMA_P(2, 0); DMA_P(2, 1); DMA_P(2, 2); DMA_P(2, 3);
  bf16x8 wo[32];
  const unsigned wto = WS_WOT + grp * 262144 + (hs * 128 + tt) * 1024;
#define WO_LD(ks) do { wo[ks] = __builtin_bit_cast(bf16x8, __builtin_amdgcn_raw_buffer_load_b128(rsWS, kvl, wto + (ks) * 4096, WOAUX)); asm volatile("" ::: "memory"); } while (0)

  f32x16 o = {}, sa, sb;
  u32x4 p0, p1 = {0u, 0u, 0u, 0u};
  const lptr fb = lds + hs * 16384 + lane * 16;
  lptr fbh = fb + 2 * SLOT; PIN(fbh);
#define SL(s) ((((s) + 1) & 2) ? fbh + (((s) + 1) & 1) * SLOT : fb + (((s) + 1) & 1) * SLOT)
  k0 = ldsr(SL(0)); k1 = ldsr(SL(0) + 1024);
  sa = MFMA(k0, q0, zero); sa = MFMA(k1, q1, sa);
  k0 = ldsr(SL(0) + 4096); k1 = ldsr(SL(0) + 4096 + 1024); v0 = ldsr(SL(0) + 2048); v1 = v0;
  unsigned long long oflag = 0;
#define EX(S, i) do { S[i] = __builtin_amdgcn_exp2f(S[i]); } while (0)
#define STEP(S, T, KP, V1P, V0P, PRIO, MEM0, MEM1) do { \
    SBAR(); SETPRIO(PRIO); EX(S, 0); EX(S, 1); PIN(S); SBAR(); \
    T = MFMA(k0, q0, zero); k0 = ldsr(KP); SBAR(); \
    EX(S, 2); p0[0] = cvtpk(S[0], S[1]); EX(S, 3); EX(S, 4); p0[1] = cvtpk(S[2], S[3]); EX(S, 5); PIN(S); PIN(p0); SBAR(); \
    T = MFMA(k1, q1, T); k1 = ldsr((KP) + 1024); SBAR(); MEM0; SBAR(); \
    EX(S, 6); p0[2] = cvtpk(S[4], S[5]); EX(S, 7); EX(S, 8); p0[3] = cvtpk(S[6], S[7]); EX(S, 9); PIN(S); PIN(p0); SBAR(); \
    o = MFMA(v1, __builtin_bit_cast(bf16x8, p1), o); v1 = ldsr(V1P); SBAR(); \
    EX(S, 10); p1[0] = cvtpk(S[8], S[9]); EX(S, 11); EX(S, 12); p1[1] = cvtpk(S[10], S[11]); EX(S, 13); PIN(S); PIN(p1); SBAR(); \
    o = MFMA(v0, __builtin_bit_cast(bf16x8, p0), o); v0 = ldsr(V0P); SBAR(); MEM1; SBAR(); \
    EX(S, 14); p1[2] = cvtpk(S[12], S[13]); EX(S, 15); p1[3] = cvtpk(S[14], S[15]); PIN(p1); SBAR(); } while (0)
#define STAGE(s) do { \
    STEP(sa, sb, SL(s) + 2 * 4096, SL(s) + 3072, SL(s) + 4096 + 2048, 1, if ((s) >= 1 && (s) <= 13) DMA_P((s) + 2, 2), WO_LD(2 * (s))); \
    STEP(sb, sa, SL(s) + 3 * 4096, SL(s) + 4096 + 3072, SL(s) + 2 * 4096 + 2048, 0, if ((s) >= 1 && (s) <= 13) DMA_P((s) + 2, 3), (void)0); \
    if ((s) < 15) asm volatile("s_waitcnt vmcnt(%0)\n\ts_barrier" ::"n"((s) == 0 ? 5 : (s) <= 13 ? 6 : 2) : "memory"); \
    STEP(sa, sb, SL((s) + 1), SL(s) + 2 * 4096 + 3072, SL(s) + 3 * 4096 + 2048, 3, if ((s) <= 12) DMA_P((s) + 3, 0), WO_LD(2 * (s) + 1)); \
    STEP(sb, sa, SL((s) + 1) + 4096, SL(s) + 3 * 4096 + 3072, SL((s) + 1) + 2048, 2, if ((s) <= 12) DMA_P((s) + 3, 1), \
         if ((s) == 14) { oflag = __hip_atomic_load((gu64*)(ws + WS_FLAG + ((lane & 15) * 16 + j) * 128), RLX_AGENT); asm volatile("" ::: "memory"); }); } while (0)
  STAGE(0); STAGE(1); STAGE(2); STAGE(3); STAGE(4); STAGE(5); STAGE(6); STAGE(7);
  STAGE(8); STAGE(9); STAGE(10); STAGE(11); STAGE(12); STAGE(13); STAGE(14); STAGE(15);
  SETPRIO(0);
  o = MFMA(v1, __builtin_bit_cast(bf16x8, p1), o); PIN(o);
  STAMP(6);

  for (unsigned spins = 0; !__all(oflag == epoch) && spins < SPIN_CAP; ++spins) {
    __builtin_amdgcn_s_sleep(2);
    oflag = __hip_atomic_load((gu64*)(ws + WS_FLAG + ((lane & 15) * 16 + j) * 128), RLX_AGENT);
  }
  asm volatile("" ::: "memory");
  float l;
  { auto r = __builtin_amdgcn_permlane32_swap(__float_as_uint(o[12]), __float_as_uint(o[12]), false, false); l = __uint_as_float(r[1]); }
  { float amax = l;
#pragma unroll
    for (int i = 0; i < 16; ++i) amax = fmaxf(amax, fabsf(o[i]));
    bool bad = !(amax < 1e30f) || !(l > 1e-30f);
#pragma unroll
    for (int i = 0; i < 16; ++i) bad |= (o[i] != o[i]);
    if (__builtin_expect(__any(bad), 0)) {
      float m = -INFINITY; o = zero;
      for (int t = 0; t < 64; ++t) {
        const unsigned ko = WS_KV + (grp * 16 + (t >> 2)) * SLOT + (hs * 4 + (t & 3)) * 4096;
        const bf16x8 a0 = __builtin_bit_cast(bf16x8, __builtin_amdgcn_raw_buffer_load_b128(rsWS, kvl, ko, 16));
        const bf16x8 a1 = __builtin_bit_cast(bf16x8, __builtin_amdgcn_raw_buffer_load_b128(rsWS, kvl + 1024, ko, 16));
        const bf16x8 b0 = __builtin_bit_cast(bf16x8, __builtin_amdgcn_raw_buffer_load_b128(rsWS, kvl + 2048, ko, 16));
        const bf16x8 b1 = __builtin_bit_cast(bf16x8, __builtin_amdgcn_raw_buffer_load_b128(rsWS, kvl + 3072, ko, 16));
        f32x16 s = MFMA(a0, q0, zero); s = MFMA(a1, q1z, s);
        const float mn = fmaxf(m, rowmax16(s)), al = __builtin_amdgcn_exp2f(m - mn); m = mn;
#pragma unroll
        for (int i = 0; i < 16; ++i) { o[i] *= al; s[i] = __builtin_amdgcn_exp2f(s[i] - mn); }
        o = MFMA(b0, pack8(s, 0), o); o = MFMA(b1, pack8(s, 1), o);
      }
      auto r = __builtin_amdgcn_permlane32_swap(__float_as_uint(o[12]), __float_as_uint(o[12]), false, false); l = __uint_as_float(r[1]);
    } }
  { const float inv = 1.0f / l;
#pragma unroll
    for (int i = 0; i < 16; ++i) o[i] *= inv;
    u32x4 a = __builtin_bit_cast(u32x4, pack8(o, 0)), b = __builtin_bit_cast(u32x4, pack8(o, 1));
    b[2] = hh ? 0u : b[2]; b[3] = hh ? 0u : b[3];
#pragma unroll
    for (int i = 0; i < 4; ++i) { a[i] = min(a[i], 0xfffffffeu); b[i] = min(b[i], 0xfffffffeu); }
    __builtin_amdgcn_raw_buffer_store_b128(a, rsWS, lane * 16, op_own, 16); __builtin_amdgcn_raw_buffer_store_b128(b, rsWS, lane * 16 + 1024, op_own, 16); }
  STAMP(7);

  { const unsigned po = WS_OP + (j * 4 + tq) * 65536 + w * 8192; u32x4 pl[8];
    for (unsigned spins = 0; spins < SPIN_CAP; ++spins) {
      unsigned mx = 0;
#pragma unroll
      for (int i = 0; i < 8; ++i) pl[i] = __builtin_amdgcn_raw_buffer_load_b128(rsWS, kvl, po + 1024 * i, 16);
#pragma unroll
      for (int i = 0; i < 8; ++i) mx = max(max(mx, max(pl[i][0], pl[i][1])), max(pl[i][2], pl[i][3]));
      if (!__any(mx == ~0u)) break;
      asm volatile("" ::: "memory");
    }
#pragma unroll
    for (int i = 0; i < 8; ++i) *(LAS u32x4*)(lds + SLOT + w * 8192 + 1024 * i + lane * 16) = pl[i]; }
  STAMP(8);
  asm volatile("s_waitcnt lgkmcnt(0)\n\ts_barrier" ::: "memory");
  if (bid == 0 && tid == 0) __hip_atomic_store((gu64*)ws, epoch, RLX_AGENT);
  f32x16 y = {};
#pragma unroll
  for (int ks = 0; ks < 32; ++ks) y = MFMA(wo[ks], ldsr(lds + SLOT + hs * 32768 + ks * 1024 + lane * 16), y);
  STAMP(9);
  { const float g = *(LAS float*)(lds + LDS_G + r32 * 4), f = hs ? 1.0f - g : g;
    const lptr xb = lds + 3 * SLOT + lane * 16;
    f32x4 s0, s1, m0, m1;
#pragma unroll
    for (int i = 0; i < 4; ++i) { s0[i] = f * (hs ? y[i] : y[8 + i]); s1[i] = f * (hs ? y[4 + i] : y[12 + i]); m0[i] = f * (hs ? y[8 + i] : y[i]); m1[i] = f * (hs ? y[12 + i] : y[4 + i]); }
    *(LAS f32x4*)(xb + w * 2048) = s0; *(LAS f32x4*)(xb + w * 2048 + 1024) = s1;
    asm volatile("s_waitcnt lgkmcnt(0)\n\ts_barrier" ::: "memory");
    m0 += *(const LAS f32x4*)(xb + (w ^ 4) * 2048); m1 += *(const LAS f32x4*)(xb + (w ^ 4) * 2048 + 1024);
    if (w != 7) {
      char* op = out + (size_t)(128 * j + 32 * tq + r32) * XROW + (112 * cq + 32 * tt + 16 * hs + 4 * hh) * 2;
      *(u32x2*)op = (u32x2){cvtpk(m0[0], m0[1]), cvtpk(m0[2], m0[3])};
      *(u32x2*)(op + 16) = (u32x2){cvtpk(m1[0], m1[1]), cvtpk(m1[2], m1[3])};
    } }
}

extern "C" void kernel_launch(void* const* d_in, const int* in_sizes, int n_in, void* d_out, int out_size, void* d_ws, size_t ws_size,
                              hipStream_t stream) {
  gated_mha_fused<<<256, 512, 0, stream>>>((const char*)d_in[0], (const char*)d_in[1], (const char*)d_in[2], (const char*)d_in[4],
                                           (const char*)d_in[5], (const char*)d_in[6], (char*)d_out, (char*)d_ws);
}
```

```cpp
#include <hip/hip_runtime.h>
#include <hip/hip_bf16.h>
#include <stdint.h>

typedef short    bf16x8 __attribute__((ext_vector_type(8)));
typedef short    s16x4  __attribute__((ext_vector_type(4)));
typedef float    f32x16 __attribute__((ext_vector_type(16)));
typedef float    f32x2  __attribute__((ext_vector_type(2)));
typedef float    f32x4  __attribute__((ext_vector_type(4)));
typedef __bf16   bf16x2 __attribute__((ext_vector_type(2)));
typedef unsigned u32x4  __attribute__((ext_vector_type(4)));
typedef unsigned u32x2  __attribute__((ext_vector_type(2)));
typedef __amdgpu_buffer_rsrc_t rsrc_t;
#define DI __device__ __forceinline__
#define LAS __attribute__((address_space(3)))
#define GAS __attribute__((address_space(1)))
typedef LAS char* lptr;
typedef GAS unsigned long long gu64;

#define KVSC "sc1"
#define WOAUX 16
#define XBYTES (SEQ * XROW)
#define WBYTES (DM * WROW)
#define SETPRIO(p) __builtin_amdgcn_s_setprio(p)
#define MFMA(a, b, c) __builtin_amdgcn_mfma_f32_32x32x16_bf16((a), (b), (c), 0, 0, 0)
#define SBAR() __builtin_amdgcn_sched_barrier(0)
#define PIN(x) asm volatile("" : "+v"(x))
#define WAIT_BAR(N) asm volatile("s_waitcnt vmcnt(" #N ") lgkmcnt(0)\n\ts_barrier" ::: "memory")
#define RLX_AGENT __ATOMIC_RELAXED, __HIP_MEMORY_SCOPE_AGENT

constexpr int SEQ = 2048, DM = 448, XROW = DM * 2, WROW = 3 * DM * 2;
constexpr unsigned WS_FLAG = 4096, WS_FLAG2 = WS_FLAG + 65536, WS_GRP = 2u << 20, GRP_WOT = 512u << 10, GRP_PAD = 768u << 10, WS_OP = WS_GRP + (16u << 20), WS_END = WS_OP + (4u << 20);
constexpr unsigned WS_DBG = WS_END;
constexpr int SLOT = 32768, LDS_MISC = 4 * SLOT, LDS_GW = LDS_MISC + 8192, LDS_JUNK = LDS_MISC + 16384, LDS_BYTES = LDS_JUNK + 256;
#define FLAGAT 6
constexpr unsigned SPIN_CAP = 1u << 18;
constexpr float QSCALE = 0.18898223650461362f * 1.4426950408889634f;

DI unsigned cvtpk(float lo, float hi) { f32x2 v = {lo, hi}; return __builtin_bit_cast(unsigned, __builtin_convertvector(v, bf16x2)); }
DI bf16x8 pack8(const f32x16& x, int s) {
  u32x4 p = {cvtpk(x[8 * s], x[8 * s + 1]), cvtpk(x[8 * s + 2], x[8 * s + 3]), cvtpk(x[8 * s + 4], x[8 * s + 5]), cvtpk(x[8 * s + 6], x[8 * s + 7])};
  return __builtin_bit_cast(bf16x8, p);
}
DI void dma16(unsigned lds_dst, unsigned voff, rsrc_t rs, unsigned soff) {
  asm volatile("s_mov_b32 m0, %0\n\ts_nop 0\n\tbuffer_load_dwordx4 %1, %2, %3 offen lds" ::"s"(lds_dst), "v"(voff), "s"(rs), "s"(soff) : "memory");
}
DI void dma16_dev(unsigned lds_dst, unsigned voff, rsrc_t rs, unsigned soff) {
  asm volatile("s_mov_b32 m0, %0\n\ts_nop 0\n\tbuffer_load_dwordx4 %1, %2, %3 offen " KVSC " lds" ::"s"(lds_dst), "v"(voff), "s"(rs), "s"(soff) : "memory");
}
DI void await_flags(unsigned long long& f, gu64* p, unsigned long long epoch, unsigned long long need) {
  unsigned n;
  asm volatile("s_mov_b32 %1, 0\n"
               "1:\n\tv_cmp_ne_u64 vcc, %0, %3\n\ts_and_b64 vcc, vcc, %4\n\ts_cbranch_scc0 2f\n\t"
               "s_sleep 1\n\tglobal_load_dwordx2 %0, %2, off sc1\n\ts_add_u32 %1, %1, 1\n\ts_cmp_lt_u32 %1, %5\n\ts_waitcnt vmcnt(0)\n\ts_cbranch_scc1 1b\n"
               "2:" : "+v"(f), "=&s"(n) : "v"(p), "v"(epoch), "s"(need), "s"(SPIN_CAP) : "vcc", "scc", "memory");
}
DI bf16x8 ldsr(lptr p) { return *(const LAS bf16x8*)p; }
DI s16x4 trr(lptr p) { return __builtin_amdgcn_ds_read_tr16_b64_v4i16((LAS s16x4*)p); }
DI bf16x8 trr2(lptr p, int second) { s16x4 lo = trr(p), hi = trr(p + second); return __builtin_shufflevector(lo, hi, 0, 1, 2, 3, 4, 5, 6, 7); }
DI float swap_max(float m) { auto r = __builtin_amdgcn_permlane32_swap(__float_as_uint(m), __float_as_uint(m), false, false); return fmaxf(__uint_as_float(r[0]), __uint_as_float(r[1])); }
DI float rowmax16(const f32x16& s) {
  float a = fmaxf(fmaxf(s[0], s[1]), s[2]), b = fmaxf(fmaxf(s[3], s[4]), s[5]);
  a = fmaxf(fmaxf(a, s[6]), s[7]); b = fmaxf(fmaxf(b, s[8]), s[9]); a = fmaxf(fmaxf(a, s[10]), s[11]); b = fmaxf(fmaxf(b, s[12]), s[13]);
  a = fmaxf(fmaxf(a, s[14]), s[15]);
  return swap_max(fmaxf(a, b));
}
DI float dot2(unsigned a, unsigned b, float c) { return __builtin_amdgcn_fdot2_f32_bf16(__builtin_bit_cast(bf16x2, a), __builtin_bit_cast(bf16x2, b), c, false); }
DI float bflo(unsigned u) { return __uint_as_float(u << 16); }
DI float bfhi(unsigned u) { return __uint_as_float(u & 0xffff0000u); }

#define STAMP(i) do { } while (0)
#define ST1(i) do { } while (0)
#define ST2(i) do { } while (0)

__global__ __launch_bounds__(512, 2) void gated_mha_fused(const char* __restrict__ x, const char* __restrict__ wqkvA, const char* __restrict__ woA,
                                                          const char* __restrict__ wqkvB, const char* __restrict__ woB, const char* __restrict__ gate,
                                                          char* __restrict__ out, char* ws) {
  __shared__ __attribute__((aligned(1024))) char lds_raw[LDS_BYTES];
  STAMP(0);
  const lptr lds = (lptr)lds_raw;
  const unsigned lds0 = (unsigned)(uintptr_t)lds;
  const int tid = threadIdx.x, lane = tid & 63, r32 = lane & 31, hh = lane >> 5;
  const int w = __builtin_amdgcn_readfirstlane(tid >> 6), tt = w & 3, hs = w >> 2;
  const int bid = blockIdx.x, grp = (bid & 7) * 2 + ((bid >> 3) & 1), j = bid >> 4, br = grp >> 3, hp = grp & 7;

  const int gp = bid & 7, pi = (grp & 1) * 16 + j;
  const int ot = 8 * gp + (pi >> 2), cq = pi & 3, gpw = 2 * cq + (gp & 1);
  const unsigned gbase = WS_GRP + (grp << 20), wbase = WS_GRP + (gpw << 21) + GRP_WOT + w * 32768;
  const rsrc_t rsX = __builtin_amdgcn_make_buffer_rsrc((void*)x, 0, XBYTES, 0x00020000);
  const rsrc_t rsW = __builtin_amdgcn_make_buffer_rsrc((void*)(br ? wqkvB : wqkvA), 0, WBYTES, 0x00020000);
  const rsrc_t rsWo = __builtin_amdgcn_make_buffer_rsrc((void*)(hs ? woB : woA), 0, DM * XROW, 0x00020000);
  const rsrc_t rsG = __builtin_amdgcn_make_buffer_rsrc((void*)gate, 0, XROW, 0x00020000);
  const rsrc_t rsWS = __builtin_amdgcn_make_buffer_rsrc((void*)ws, 0, WS_END, 0x00020000);

  const unsigned xrow = 16 * w + (lane >> 3);
  const unsigned xoff0 = (128 * j + xrow) * XROW + 16 * ((lane & 7) ^ ((lane >> 4) & 7));
  const unsigned xoff1 = (128 * j + xrow + 8) * XROW + 16 * ((lane & 7) ^ ((4 + (lane >> 4)) & 7));
  const unsigned wl = (lane >> 3) * WROW + (((lane >> 2) ^ (lane >> 4)) & 1) * 56 + 16 * (lane & 3) + hp * 112;
  const unsigned wkv_s = 16 * tt * WROW + (hs + 1) * XROW;
#define DMA_KV(c, i) do { const unsigned sl_ = lds0 + ((c) & 3) * SLOT + 2048 * w + ((i) >> 1) * 1024; \
    if ((i) & 1) dma16(sl_ + 16384, wl, rsW, wkv_s + (64 * (c) + 8 * ((i) >> 1)) * WROW); else dma16(sl_, ((i) >> 1) ? xoff1 : xoff0, rsX, 128 * (c)); } while (0)
#define DMA_KV4(c) do { DMA_KV(c, 0); DMA_KV(c, 1); DMA_KV(c, 2); DMA_KV(c, 3); } while (0)
#define DMA_QA(q) dma16(lds0 + 3 * SLOT + 4096 * w + 1024 * (q), wl, rsW, (32 * w + 8 * (q)) * WROW)
#define DMA_QB(q) dma16(lds0 + 3072 * w + 1024 * (q), wl, rsW, (256 + 24 * w + 8 * (q)) * WROW)
  const unsigned op_own = WS_OP + ((j * 4 + tt) * 32 + 2 * grp + hs) * 2048;
  DMA_KV4(0);
  unsigned gw;
  asm volatile("buffer_load_ushort %0, %1, %2, 0 offen" : "=v"(gw) : "v"(tid * 2), "s"(rsG) : "memory");
  {
    const char* tp = w == 0 ? x + (size_t)min(bid * 56 + lane, 14335) * 128
                   : w < 3 ? (w == 1 ? wqkvA : wqkvB) + (size_t)min(bid * 37 + min(lane, 36), 9407) * 128
                   : w < 5 ? (w == 3 ? woA : woB) + (size_t)min(bid * 13 + min(lane, 12), 3135) * 128
                   : w < 7 ? ws + (w == 5 ? WS_GRP + ((hh ? gpw : gp) << 21) + GRP_PAD : hh ? op_own : WS_OP + ot * 65536) : out + (size_t)(32 * ot + 31 * hh) * XROW;
    asm volatile("s_mov_b32 m0, %0\n\ts_nop 0\n\tglobal_load_lds_dword %1, off sc1" ::"s"(lds0 + LDS_JUNK), "v"(tp) : "memory"); }
  DMA_KV4(1);
  unsigned long long epoch = __hip_atomic_load((gu64*)ws, RLX_AGENT);
  asm volatile("" ::: "memory");
#define DMA_WO() dma16(lds0 + LDS_MISC + 1024 * w, (lane >> 2) * XROW + 16 * (lane & 3), rsWo, (28 * (4 * tt + (pi >> 3)) + 16 * ((pi >> 2) & 1)) * XROW + 224 * (gp >> 1) + 64 * (pi & 3))

  bf16x8 xf[28], wk[2], wv[2];
  f32x16 accK = {}, accV = {};
  const unsigned xr = (32 * tt + r32) * 128 + ((hh ^ ((r32 >> 1) & 7)) * 16);
  const unsigned trl = (8 * hh + ((lane & 15) >> 2)) * 128 + ((hs ^ (lane >> 3)) & 1) * 64 + ((lane >> 4) & 1) * 32 + (lane & 3) * 8;
  lptr ldh = lds + 2 * SLOT; PIN(ldh);
#define KSL(c) ((((c) & 2) ? ldh : lds) + ((c) & 1) * SLOT)
  const bool gl = (lane & 19) == 19;
  const unsigned kdl = gl ? 8u : (unsigned)SLOT, kwrap = gl ? 8u : (unsigned)(-3 * SLOT);
  lptr ak = lds + (gl ? LDS_GW + (trl & ~127u) : 16384 + trl);
#define KV_RD(n) do { const lptr s_ = KSL((n) >> 2); xf[n] = ldsr(s_ + (xr ^ (32 * ((n) & 3)))); \
    if ((n) && !((n) & 3)) { ak += (n) == 16 ? kwrap : kdl; PIN(ak); } \
    wk[(n) & 1] = trr2(ak + ((n) & 3) * 2048, 512); wv[(n) & 1] = trr2(s_ + 24576 + trl + ((n) & 3) * 2048, 512); } while (0)
#define KV_STEP(n, MEM) do { SBAR(); if ((n) < 27) KV_RD((n) + 1); SBAR(); \
    accK = MFMA(wk[(n) & 1], xf[n], accK); accV = MFMA(xf[n], wv[(n) & 1], accV); SBAR(); MEM; SBAR(); } while (0)
  asm volatile("s_waitcnt vmcnt(6)" : "+v"(gw) :: "memory");
  *(LAS unsigned short*)(lds + LDS_GW + lane * 128 + w * 8) = (unsigned short)gw;
  WAIT_BAR(6); STAMP(1); ST1(1); KV_RD(0);
  KV_STEP(0, DMA_KV(2, 0); DMA_KV(2, 1)); KV_STEP(1, DMA_KV(2, 2)); KV_STEP(2, DMA_KV(2, 3)); WAIT_BAR(5); ST1(2);
  KV_STEP(3, DMA_KV(3, 0)); KV_STEP(4, DMA_KV(3, 1)); KV_STEP(5, DMA_KV(3, 2)); KV_STEP(6, DMA_KV(3, 3)); WAIT_BAR(4); ST1(3);
  KV_STEP(7, DMA_KV(4, 0)); KV_STEP(8, DMA_KV(4, 1)); KV_STEP(9, DMA_KV(4, 2)); KV_STEP(10, DMA_KV(4, 3)); WAIT_BAR(4); ST1(4);
  KV_STEP(11, DMA_KV(5, 0)); KV_STEP(12, DMA_KV(5, 1)); KV_STEP(13, DMA_KV(5, 2)); KV_STEP(14, DMA_KV(5, 3)); WAIT_BAR(4); ST1(5);
  KV_STEP(15, DMA_KV(6, 0)); KV_STEP(16, DMA_KV(6, 1)); KV_STEP(17, DMA_KV(6, 2)); KV_STEP(18, DMA_KV(6, 3)); WAIT_BAR(4); ST1(6);
  KV_STEP(19, DMA_QA(0)); KV_STEP(20, (void)0); KV_STEP(21, DMA_QA(1)); KV_STEP(22, (void)0); WAIT_BAR(2); ST1(7);
  KV_STEP(23, DMA_QA(2)); KV_STEP(24, (void)0); KV_STEP(25, DMA_QA(3)); KV_STEP(26, (void)0); KV_STEP(27, (void)0);
  STAMP(2); ST1(8);
  bf16x8 k0 = pack8(accK, 0), k1 = pack8(accK, 1), v0 = pack8(accV, 0), v1 = pack8(accV, 1);
  { u32x4 t = __builtin_bit_cast(u32x4, k1); t[2] = hh ? 0x00003f80u : t[2]; t[3] = hh ? 0u : t[3]; k1 = __builtin_bit_cast(bf16x8, t);
    u32x4 a = __builtin_bit_cast(u32x4, v0), b = __builtin_bit_cast(u32x4, v1); const unsigned pv = r32 == 28 ? 0x3f803f80u : 0u;
#pragma unroll
    for (int i = 0; i < 4; ++i) { a[i] = r32 >= 28 ? pv : a[i]; b[i] = r32 >= 28 ? pv : b[i]; }
    v0 = __builtin_bit_cast(bf16x8, a); v1 = __builtin_bit_cast(bf16x8, b); }
  { const unsigned kvo = gbase + j * SLOT + w * 4096;
    __builtin_amdgcn_raw_buffer_store_b128(__builtin_bit_cast(u32x4, k0), rsWS, lane * 16, kvo, 16);
    __builtin_amdgcn_raw_buffer_store_b128(__builtin_bit_cast(u32x4, k1), rsWS, lane * 16 + 1024, kvo, 16);
    __builtin_amdgcn_raw_buffer_store_b128(__builtin_bit_cast(u32x4, v0), rsWS, lane * 16 + 2048, kvo, 16);
    __builtin_amdgcn_raw_buffer_store_b128(__builtin_bit_cast(u32x4, v1), rsWS, lane * 16 + 3072, kvo, 16);
    const lptr own = lds + SLOT + w * 4096 + lane * 16;
    *(LAS bf16x8*)own = k0; *(LAS bf16x8*)(own + 1024) = k1; *(LAS bf16x8*)(own + 2048) = v0; *(LAS bf16x8*)(own + 3072) = v1; }
  float gs;
  { auto r = __builtin_amdgcn_permlane32_swap(__float_as_uint(accK[12]), __float_as_uint(accK[12]), false, false);
    gs = 1.0f / (1.0f + __expf(-__uint_as_float(r[1]))); gs = br ? 1.0f - gs : gs; }
  f32x16 accQ = {};
  WAIT_BAR(4); ST1(9);
#pragma unroll
  for (int ks = 0; ks < FLAGAT; ++ks) accQ = MFMA(trr2(ldh + SLOT + trl + ks * 2048, 512), xf[ks], accQ);
  PIN(accQ);
  asm volatile("s_waitcnt vmcnt(0)" ::: "memory");
  PIN(epoch); epoch += 1;
  if (lane == 0) __hip_atomic_store((gu64*)(ws + WS_FLAG + (gp * 8 + w) * 1024 + pi * 32), epoch, RLX_AGENT);
  DMA_QB(0); DMA_QB(1); DMA_QB(2); DMA_WO();
  ST1(10);
#pragma unroll
  for (int ks = FLAGAT; ks < 16; ++ks) accQ = MFMA(trr2(ldh + SLOT + trl + ks * 2048, 512), xf[ks], accQ);
  PIN(accQ);
  gu64* fp = (gu64*)(ws + WS_FLAG + (gp * 8 + w) * 1024 + ((grp & 1) * 16 + (lane & 15)) * 32);
  STAMP(3);
  WAIT_BAR(1);
  unsigned long long kflag;
#define POLL() asm volatile("global_load_dwordx2 %0, %1, off sc1" : "=v"(kflag) : "v"(fp) : "memory")
  POLL();
#pragma unroll
  for (int ks = 0; ks < 12; ++ks) accQ = MFMA(trr2(lds + trl + ks * 2048, 512), xf[16 + ks], accQ);
#pragma unroll
  for (int i = 0; i < 16; ++i) accQ[i] *= QSCALE;
  bf16x8 q0 = pack8(accQ, 0), q1 = pack8(accQ, 1);
  { u32x4 t = __builtin_bit_cast(u32x4, q1); t[2] = hh ? 0u : t[2]; t[3] = hh ? 0u : t[3]; q1 = __builtin_bit_cast(bf16x8, t); }
  const bf16x8 q1z = q1;
  const f32x16 zero = {};
  {
    f32x16 s = MFMA(k0, q0, zero); s = MFMA(k1, q1, s);
    const float m = rowmax16(s);
    u32x4 t = __builtin_bit_cast(u32x4, q1); t[2] = hh ? (cvtpk(-m, 0.f) & 0xffffu) : t[2]; q1 = __builtin_bit_cast(bf16x8, t); }
  PIN(q1);
  STAMP(4);

  asm volatile("s_waitcnt vmcnt(0)" ::: "memory");
  await_flags(kflag, fp, epoch, (j < 3 ? 15ull : 7ull) & ~(1ull << j));
  STAMP(5); ST2(1);
  bf16x8 wot;
  { const lptr wp = lds + LDS_MISC + w * 1024 + (4 * hh + ((lane & 15) >> 2)) * 64 + ((lane >> 4) & 1) * 32 + (lane & 3) * 8; wot = trr2(wp, 512); }
  const unsigned kvl = lane * 16, kvd = lds0 + w * 4096, kvs = gbase + w * 4096;
#define DMA_P(s, p) dma16_dev(kvd + (((s) + 1) & 3) * SLOT + (p) * 1024, kvl, rsWS, kvs + ((s) - 1 + ((s) - 1 >= j)) * SLOT + (p) * 1024)
  DMA_P(1, 0); DMA_P(1, 1); DMA_P(1, 2); DMA_P(1, 3); DMA_P(2, 0); DMA_P(2, 1); DMA_P(2, 2); DMA_P(2, 3);
  bf16x8 wo[32];
#define WO_LD(ks) do { wo[ks] = __builtin_bit_cast(bf16x8, __builtin_amdgcn_raw_buffer_load_b128(rsWS, kvl, wbase + (ks) * 1024, WOAUX)); asm volatile("" ::: "memory"); } while (0)

  f32x16 o = {}, sa, sb;
  u32x4 p0, p1 = {0u, 0u, 0u, 0u};
  const lptr fb = lds + hs * 16384 + lane * 16;
  lptr fbh = fb + 2 * SLOT; PIN(fbh);
#define SL(s) ((((s) + 1) & 2) ? fbh + (((s) + 1) & 1) * SLOT : fb + (((s) + 1) & 1) * SLOT)
  k0 = ldsr(SL(0)); k1 = ldsr(SL(0) + 1024);
  sa = MFMA(k0, q0, zero); sa = MFMA(k1, q1, sa);
  k0 = ldsr(SL(0) + 4096); k1 = ldsr(SL(0) + 4096 + 1024); v0 = ldsr(SL(0) + 2048); v1 = v0;
#define EX(S, i) do { S[i] = __builtin_amdgcn_exp2f(S[i]); } while (0)
#define STEP(S, T, KP, V1P, V0P, PRIO, MEM0, MEM1) do { \
    SBAR(); SETPRIO(PRIO); EX(S, 0); EX(S, 1); PIN(S); SBAR(); \
    T = MFMA(k0, q0, zero); k0 = ldsr(KP); SBAR(); \
    EX(S, 2); p0[0] = cvtpk(S[0], S[1]); EX(S, 3); EX(S, 4); p0[1] = cvtpk(S[2], S[3]); EX(S, 5); PIN(S); PIN(p0); SBAR(); \
    T = MFMA(k1, q1, T); k1 = ldsr((KP) + 1024); SBAR(); MEM0; SBAR(); \
    EX(S, 6); p0[2] = cvtpk(S[4], S[5]); EX(S, 7); EX(S, 8); p0[3] = cvtpk(S[6], S[7]); EX(S, 9); PIN(S); PIN(p0); SBAR(); \
    o = MFMA(v1, __builtin_bit_cast(bf16x8, p1), o); v1 = ldsr(V1P); SBAR(); \
    EX(S, 10); p1[0] = cvtpk(S[8], S[9]); EX(S, 11); EX(S, 12); p1[1] = cvtpk(S[10], S[11]); EX(S, 13); PIN(S); PIN(p1); SBAR(); \
    o = MFMA(v0, __builtin_bit_cast(bf16x8, p0), o); v0 = ldsr(V0P); SBAR(); MEM1; SBAR(); \
    EX(S, 14); p1[2] = cvtpk(S[12], S[13]); EX(S, 15); p1[3] = cvtpk(S[14], S[15]); PIN(p1); SBAR(); } while (0)
  const u32x4 sent = {~0u, ~0u, ~0u, ~0u};
#define SENT(i) do { __builtin_amdgcn_raw_buffer_store_b128(sent, rsWS, kvl + 1024 * (i), op_own, 16); asm volatile("" ::: "memory"); } while (0)
#define WOT_ST() do { __builtin_amdgcn_raw_buffer_store_b128(__builtin_bit_cast(u32x4, wot), rsWS, kvl, WS_GRP + (gp << 21) + GRP_WOT + w * 32768 + pi * 1024, 16); asm volatile("" ::: "memory"); } while (0)
#define STAGE(s) do { \
    STEP(sa, sb, SL(s) + 2 * 4096, SL(s) + 3072, SL(s) + 4096 + 2048, 1, if ((s) == 0) SENT(0); else if ((s) <= 13) DMA_P((s) + 2, 2), \
         if ((s) >= 5 && (s) <= 14) WO_LD(3 * (s) - 13); else if ((s) == 0) WOT_ST()); \
    STEP(sb, sa, SL(s) + 3 * 4096, SL(s) + 4096 + 3072, SL(s) + 2 * 4096 + 2048, 0, if ((s) == 0) SENT(1); else if ((s) <= 13) DMA_P((s) + 2, 3), \
         if ((s) >= 5 && (s) <= 14) WO_LD(3 * (s) - 12); else if ((s) == 0 || (s) == 3) POLL()); \
    asm volatile("s_waitcnt vmcnt(%0)\n\ts_barrier" ::"n"((s) == 0 ? 8 : (s) == 1 ? 4 : (s) <= 3 ? 5 : (s) == 4 ? 4 : (s) <= 13 ? 8 : (s) == 14 ? 4 : 63) : "memory"); \
    if ((s) < 3) ST2(2 + (s)); \
    if ((s) == 1) { await_flags(kflag, fp, epoch, 0xffffull); \
        \
      if (lane == 0) __hip_atomic_store((gu64*)(ws + WS_FLAG2 + (gp * 8 + w) * 1024 + pi * 32), epoch, RLX_AGENT); \
      fp = (gu64*)(ws + WS_FLAG2 + (hh ? (gpw * 8 + w) * 1024 + r32 * 32 : (w * 8 + (lane & 1) * 4 + (ot & 3)) * 1024 + ((lane & 2) * 8 + (ot >> 2)) * 32)); } \
    if ((s) == 4) await_flags(kflag, fp, epoch, ~0ull); \
    STEP(sa, sb, SL((s) + 1), SL(s) + 2 * 4096 + 3072, SL(s) + 3 * 4096 + 2048, 3, if ((s) <= 12) DMA_P((s) + 3, 0), if ((s) >= 4 && (s) <= 14) WO_LD((s) == 4 ? 0 : 3 * (s) - 11)); \
    STEP(sb, sa, SL((s) + 1) + 4096, SL(s) + 3 * 4096 + 3072, SL((s) + 1) + 2048, 2, if ((s) <= 12) DMA_P((s) + 3, 1), if ((s) == 4) WO_LD(1)); } while (0)
  STAGE(0); STAGE(1); STAGE(2); STAGE(3); STAGE(4); STAGE(5); STAGE(6); STAGE(7);
  STAGE(8); STAGE(9); STAGE(10); STAGE(11); STAGE(12); STAGE(13); STAGE(14); STAGE(15);
  SETPRIO(0);
  o = MFMA(v1, __builtin_bit_cast(bf16x8, p1), o); PIN(o);
  STAMP(6); ST2(5);

  float l;
  { auto r = __builtin_amdgcn_permlane32_swap(__float_as_uint(o[12]), __float_as_uint(o[12]), false, false); l = __uint_as_float(r[1]); }
  { float amax = l;
#pragma unroll
    for (int i = 0; i < 16; ++i) amax = fmaxf(amax, fabsf(o[i]));
    bool bad = !(amax < 1e30f) || !(l > 1e-30f);
#pragma unroll
    for (int i = 0; i < 16; ++i) bad |= (o[i] != o[i]);
    if (__builtin_expect(__any(bad), 0)) {
      float m = -INFINITY; o = zero;
      for (int t = 0; t < 64; ++t) {
        const unsigned ko = gbase + (t >> 2) * SLOT + (hs * 4 + (t & 3)) * 4096;
        const bf16x8 a0 = __builtin_bit_cast(bf16x8, __builtin_amdgcn_raw_buffer_load_b128(rsWS, kvl, ko, 16));
        const bf16x8 a1 = __builtin_bit_cast(bf16x8, __builtin_amdgcn_raw_buffer_load_b128(rsWS, kvl + 1024, ko, 16));
        const bf16x8 b0 = __builtin_bit_cast(bf16x8, __builtin_amdgcn_raw_buffer_load_b128(rsWS, kvl + 2048, ko, 16));
        const bf16x8 b1 = __builtin_bit_cast(bf16x8, __builtin_amdgcn_raw_buffer_load_b128(rsWS, kvl + 3072, ko, 16));
        f32x16 s = MFMA(a0, q0, zero); s = MFMA(a1, q1z, s);
        const float mn = fmaxf(m, rowmax16(s)), al = __builtin_amdgcn_exp2f(m - mn); m = mn;
#pragma unroll
        for (int i = 0; i < 16; ++i) { o[i] *= al; s[i] = __builtin_amdgcn_exp2f(s[i] - mn); }
        o = MFMA(b0, pack8(s, 0), o); o = MFMA(b1, pack8(s, 1), o);
      }
      auto r = __builtin_amdgcn_permlane32_swap(__float_as_uint(o[12]), __float_as_uint(o[12]), false, false); l = __uint_as_float(r[1]);
    } }
  { const float inv = gs / l;
#pragma unroll
    for (int i = 0; i < 16; ++i) o[i] *= inv;
    u32x4 a = __builtin_bit_cast(u32x4, pack8(o, 0)), b = __builtin_bit_cast(u32x4, pack8(o, 1));
    b[2] = hh ? 0u : b[2]; b[3] = hh ? 0u : b[3];
#pragma unroll
    for (int i = 0; i < 4; ++i) { a[i] = min(a[i], 0xfffffffeu); b[i] = min(b[i], 0xfffffffeu); }
    __builtin_amdgcn_raw_buffer_store_b128(a, rsWS, lane * 16, op_own, 16); __builtin_amdgcn_raw_buffer_store_b128(b, rsWS, lane * 16 + 1024, op_own, 16); }
  __builtin_amdgcn_s_barrier();
  STAMP(7); ST2(6);

  f32x16 y[4] = {};
  const lptr rb = lds + SLOT + lane * 16;
  asm volatile("" ::: "memory"); ST2(7);
  { const unsigned po = WS_OP + ot * 65536 + w * 8192; u32x4 pl[8];
#pragma unroll
    for (int i = 0; i < 8; ++i) pl[i] = __builtin_amdgcn_raw_buffer_load_b128(rsWS, kvl, po + 1024 * i, 16);
#pragma unroll
    for (int hf = 0; hf < 2; ++hf) {
      for (unsigned spins = 0; spins < SPIN_CAP; ++spins) {
        unsigned mx = 0;
#pragma unroll
        for (int i = 4 * hf; i < 4 * hf + 4; ++i) mx = max(max(mx, max(pl[i][0], pl[i][1])), max(pl[i][2], pl[i][3]));
        if (!__any(mx == ~0u)) break;
        __builtin_amdgcn_s_sleep(4);
        asm volatile("" ::: "memory");
#pragma unroll
        for (int i = 4 * hf; i < 4 * hf + 4; ++i) pl[i] = __builtin_amdgcn_raw_buffer_load_b128(rsWS, kvl, po + 1024 * i, 16);
      }
      if (hf == 0) {
        ST2(8);
#pragma unroll
        for (int i = 0; i < 4; ++i)
#pragma unroll
          for (int nt = 0; nt < 4; ++nt) y[nt] = MFMA(wo[4 * i + nt], __builtin_bit_cast(bf16x8, pl[i]), y[nt]);
        PIN(y[0]); PIN(y[1]); PIN(y[2]); PIN(y[3]);
      } else {
#pragma unroll
        for (int nt = 0; nt < 5; ++nt) {
          SBAR();
          if (nt < 4)
#pragma unroll
            for (int i = 4; i < 8; ++i) y[nt] = MFMA(wo[4 * i + nt], __builtin_bit_cast(bf16x8, pl[i]), y[nt]);
          SBAR();
          if (nt > 0)
#pragma unroll
            for (int u = 4 * nt - 4; u < min(4 * nt, 14); ++u)
              *(LAS f32x4*)(rb + w * 14336 + u * 1024) = (f32x4){y[u >> 2][4 * (u & 3)], y[u >> 2][4 * (u & 3) + 1], y[u >> 2][4 * (u & 3) + 2], y[u >> 2][4 * (u & 3) + 3]};
        }
      }
    } }
  STAMP(8); ST2(9);
  {
    asm volatile("s_waitcnt lgkmcnt(0)\n\ts_barrier" ::: "memory");
    STAMP(9);
    if (bid == 0 && tid == 0) __hip_atomic_store((gu64*)ws, epoch, RLX_AGENT);
    if (w != 7) {
      unsigned r[4];
#pragma unroll
      for (int u = 0; u < 2; ++u) { f32x4 t[8];
#pragma unroll
        for (int k = 0; k < 8; ++k) t[k] = *(const LAS f32x4*)(rb + k * 14336 + (2 * w + u) * 1024);
        const f32x4 a = (t[0] + t[1]) + (t[2] + t[3]), b = (t[4] + t[5]) + (t[6] + t[7]), m = a + b;
        r[2 * u] = cvtpk(m[0], m[1]); r[2 * u + 1] = cvtpk(m[2], m[3]); }
      auto e0 = __builtin_amdgcn_permlane32_swap(r[0], r[2], false, false), e1 = __builtin_amdgcn_permlane32_swap(r[1], r[3], false, false);
      *(u32x4*)(out + (size_t)(32 * ot + r32) * XROW + (112 * cq + 16 * w + 8 * hh) * 2) = (u32x4){e0[0], e1[0], e0[1], e1[1]};
    } }
}

extern "C" void kernel_launch(void* const* d_in, const int* in_sizes, int n_in, void* d_out, int out_size, void* d_ws, size_t ws_size,
                              hipStream_t stream) {
  gated_mha_fused<<<256, 512, 0, stream>>>((const char*)d_in[0], (const char*)d_in[1], (const char*)d_in[2], (const char*)d_in[4],
                                           (const char*)d_in[5], (const char*)d_in[6], (char*)d_out, (char*)d_ws);
}
```

```cpp
#include <hip/hip_runtime.h>
#include <hip/hip_bf16.h>
#include <stdint.h>

typedef short    bf16x8 __attribute__((ext_vector_type(8)));
typedef short    s16x4  __attribute__((ext_vector_type(4)));
typedef float    f32x16 __attribute__((ext_vector_type(16)));
typedef float    f32x2  __attribute__((ext_vector_type(2)));
typedef float    f32x4  __attribute__((ext_vector_type(4)));
typedef __bf16   bf16x2 __attribute__((ext_vector_type(2)));
typedef unsigned u32x4  __attribute__((ext_vector_type(4)));
typedef unsigned u32x2  __attribute__((ext_vector_type(2)));
typedef __amdgpu_buffer_rsrc_t rsrc_t;
#define DI __device__ __forceinline__
#define LAS __attribute__((address_space(3)))
#define GAS __attribute__((address_space(1)))
typedef LAS char* lptr;
typedef GAS unsigned long long gu64;

#define KVSC "sc1"
#define WOAUX 16
#define XBYTES (SEQ * XROW)
#define WBYTES (DM * WROW)
#define SETPRIO(p) __builtin_amdgcn_s_setprio(p)
#define MFMA(a, b, c) __builtin_amdgcn_mfma_f32_32x32x16_bf16((a), (b), (c), 0, 0, 0)
#define SBAR() __builtin_amdgcn_sched_barrier(0)
#define PIN(x) asm volatile("" : "+v"(x))
#define WAIT_BAR(N) asm volatile("s_waitcnt vmcnt(" #N ") lgkmcnt(0)\n\ts_barrier" ::: "memory")
#define RLX_AGENT __ATOMIC_RELAXED, __HIP_MEMORY_SCOPE_AGENT

constexpr int SEQ = 2048, DM = 448, XROW = DM * 2, WROW = 3 * DM * 2;
constexpr unsigned WS_FLAG = 4096, WS_FLAG2 = WS_FLAG + 65536, WS_GRP = 2u << 20, GRP_WOT = 512u << 10, GRP_PAD = 768u << 10, WS_OP = WS_GRP + (16u << 20), WS_END = WS_OP + (4u << 20);
constexpr unsigned WS_DBG = WS_END;
constexpr int SLOT = 32768, LDS_MISC = 4 * SLOT, LDS_GW = LDS_MISC + 8192, LDS_JUNK = LDS_MISC + 16384, LDS_BYTES = LDS_JUNK + 256;
#define FLAGAT 6
constexpr unsigned SPIN_CAP = 1u << 18;
constexpr float QSCALE = 0.18898223650461362f * 1.4426950408889634f;

DI unsigned cvtpk(float lo, float hi) { f32x2 v = {lo, hi}; return __builtin_bit_cast(unsigned, __builtin_convertvector(v, bf16x2)); }
DI bf16x8 pack8(const f32x16& x, int s) {
  u32x4 p = {cvtpk(x[8 * s], x[8 * s + 1]), cvtpk(x[8 * s + 2], x[8 * s + 3]), cvtpk(x[8 * s + 4], x[8 * s + 5]), cvtpk(x[8 * s + 6], x[8 * s + 7])};
  return __builtin_bit_cast(bf16x8, p);
}
DI void dma16(unsigned lds_dst, unsigned voff, rsrc_t rs, unsigned soff) {
  asm volatile("s_mov_b32 m0, %0\n\ts_nop 0\n\tbuffer_load_dwordx4 %1, %2, %3 offen lds" ::"s"(lds_dst), "v"(voff), "s"(rs), "s"(soff) : "memory");
}
DI void dma16_dev(unsigned lds_dst, unsigned voff, rsrc_t rs, unsigned soff) {
  asm volatile("s_mov_b32 m0, %0\n\ts_nop 0\n\tbuffer_load_dwordx4 %1, %2, %3 offen " KVSC " lds" ::"s"(lds_dst), "v"(voff), "s"(rs), "s"(soff) : "memory");
}
DI void await_flags(unsigned long long& f, gu64* p, unsigned long long epoch, unsigned long long need) {
  unsigned n;
  asm volatile("s_mov_b32 %1, 0\n"
               "1:\n\tv_cmp_ne_u64 vcc, %0, %3\n\ts_and_b64 vcc, vcc, %4\n\ts_cbranch_scc0 2f\n\t"
               "s_sleep 1\n\tglobal_load_dwordx2 %0, %2, off sc1\n\ts_add_u32 %1, %1, 1\n\ts_cmp_lt_u32 %1, %5\n\ts_waitcnt vmcnt(0)\n\ts_cbranch_scc1 1b\n"
               "2:" : "+v"(f), "=&s"(n) : "v"(p), "v"(epoch), "s"(need), "s"(SPIN_CAP) : "vcc", "scc", "memory");
}
DI bf16x8 ldsr(lptr p) { return *(const LAS bf16x8*)p; }
DI s16x4 trr(lptr p) { return __builtin_amdgcn_ds_read_tr16_b64_v4i16((LAS s16x4*)p); }
DI bf16x8 trr2(lptr p, int second) { s16x4 lo = trr(p), hi = trr(p + second); return __builtin_shufflevector(lo, hi, 0, 1, 2, 3, 4, 5, 6, 7); }
DI float swap_max(float m) { auto r = __builtin_amdgcn_permlane32_swap(__float_as_uint(m), __float_as_uint(m), false, false); return fmaxf(__uint_as_float(r[0]), __uint_as_float(r[1])); }
DI float rowmax16(const f32x16& s) {
  float a = fmaxf(fmaxf(s[0], s[1]), s[2]), b = fmaxf(fmaxf(s[3], s[4]), s[5]);
  a = fmaxf(fmaxf(a, s[6]), s[7]); b = fmaxf(fmaxf(b, s[8]), s[9]); a = fmaxf(fmaxf(a, s[10]), s[11]); b = fmaxf(fmaxf(b, s[12]), s[13]);
  a = fmaxf(fmaxf(a, s[14]), s[15]);
  return swap_max(fmaxf(a, b));
}
DI float dot2(unsigned a, unsigned b, float c) { return __builtin_amdgcn_fdot2_f32_bf16(__builtin_bit_cast(bf16x2, a), __builtin_bit_cast(bf16x2, b), c, false); }
DI float bflo(unsigned u) { return __uint_as_float(u << 16); }
DI float bfhi(unsigned u) { return __uint_as_float(u & 0xffff0000u); }

#define STAMP(i) do { if ((i) == 0) { SBAR(); xa = __builtin_amdgcn_s_memrealtime(); SBAR(); } if ((i) == 0) { SBAR(); xb = __builtin_amdgcn_s_memrealtime(); SBAR(); } } while (0)
#define ST1(i) do { } while (0)
#define ST2(i) do { } while (0)

__global__ __launch_bounds__(512, 2) void gated_mha_fused(const char* __restrict__ x, const char* __restrict__ wqkvA, const char* __restrict__ woA,
                                                          const char* __restrict__ wqkvB, const char* __restrict__ woB, const char* __restrict__ gate,
                                                          char* __restrict__ out, char* ws) {
  __shared__ __attribute__((aligned(1024))) char lds_raw[LDS_BYTES];
  const unsigned long long hold0 = __builtin_amdgcn_s_memrealtime(); unsigned long long xa = 0, xb = 0;
  STAMP(0);
  const lptr lds = (lptr)lds_raw;
  const unsigned lds0 = (unsigned)(uintptr_t)lds;
  const int tid = threadIdx.x, lane = tid & 63, r32 = lane & 31, hh = lane >> 5;
  const int w = __builtin_amdgcn_readfirstlane(tid >> 6), tt = w & 3, hs = w >> 2;
  const int bid = blockIdx.x, grp = (bid & 7) * 2 + ((bid >> 3) & 1), j = bid >> 4, br = grp >> 3, hp = grp & 7;

  const int gp = bid & 7, pi = (grp & 1) * 16 + j;
  const int ot = 8 * gp + (pi >> 2), cq = pi & 3, gpw = 2 * cq + (gp & 1);
  const unsigned gbase = WS_GRP + (grp << 20), wbase = WS_GRP + (gpw << 21) + GRP_WOT + w * 32768;
  const rsrc_t rsX = __builtin_amdgcn_make_buffer_rsrc((void*)x, 0, XBYTES, 0x00020000);
  const rsrc_t rsW = __builtin_amdgcn_make_buffer_rsrc((void*)(br ? wqkvB : wqkvA), 0, WBYTES, 0x00020000);
  const rsrc_t rsWo = __builtin_amdgcn_make_buffer_rsrc((void*)(hs ? woB : woA), 0, DM * XROW, 0x00020000);
  const rsrc_t rsG = __builtin_amdgcn_make_buffer_rsrc((void*)gate, 0, XROW, 0x00020000);
  const rsrc_t rsWS = __builtin_amdgcn_make_buffer_rsrc((void*)ws, 0, WS_END, 0x00020000);

  const unsigned xrow = 16 * w + (lane >> 3);
  const unsigned xoff0 = (128 * j + xrow) * XROW + 16 * ((lane & 7) ^ ((lane >> 4) & 7));
  const unsigned xoff1 = (128 * j + xrow + 8) * XROW + 16 * ((lane & 7) ^ ((4 + (lane >> 4)) & 7));
  const unsigned wl = (lane >> 3) * WROW + (((lane >> 2) ^ (lane >> 4)) & 1) * 56 + 16 * (lane & 3) + hp * 112;
  const unsigned wkv_s = 16 * tt * WROW + (hs + 1) * XROW;
#define DMA_KV(c, i) do { const unsigned sl_ = lds0 + ((c) & 3) * SLOT + 2048 * w + ((i) >> 1) * 1024; \
    if ((i) & 1) dma16(sl_ + 16384, wl, rsW, wkv_s + (64 * (c) + 8 * ((i) >> 1)) * WROW); else dma16(sl_, ((i) >> 1) ? xoff1 : xoff0, rsX, 128 * (c)); } while (0)
#define DMA_KV4(c) do { DMA_KV(c, 0); DMA_KV(c, 1); DMA_KV(c, 2); DMA_KV(c, 3); } while (0)
#define DMA_QA(q) dma16(lds0 + 3 * SLOT + 4096 * w + 1024 * (q), wl, rsW, (32 * w + 8 * (q)) * WROW)
#define DMA_QB(q) dma16(lds0 + 3072 * w + 1024 * (q), wl, rsW, (256 + 24 * w + 8 * (q)) * WROW)
  const unsigned op_own = WS_OP + ((j * 4 + tt) * 32 + 2 * grp + hs) * 2048;
  DMA_KV4(0);
  unsigned gw;
  asm volatile("buffer_load_ushort %0, %1, %2, 0 offen" : "=v"(gw) : "v"(tid * 2), "s"(rsG) : "memory");
  {
    const char* tp = w == 0 ? x + (size_t)min(bid * 56 + lane, 14335) * 128
                   : w < 3 ? (w == 1 ? wqkvA : wqkvB) + (size_t)min(bid * 37 + min(lane, 36), 9407) * 128
                   : w < 5 ? (w == 3 ? woA : woB) + (size_t)min(bid * 13 + min(lane, 12), 3135) * 128
                   : w < 7 ? ws + (w == 5 ? WS_GRP + ((hh ? gpw : gp) << 21) + GRP_PAD : hh ? op_own : WS_OP + ot * 65536) : out + (size_t)(32 * ot + 31 * hh) * XROW;
    asm volatile("s_mov_b32 m0, %0\n\ts_nop 0\n\tglobal_load_lds_dword %1, off sc1" ::"s"(lds0 + LDS_JUNK), "v"(tp) : "memory"); }
  DMA_KV4(1);
  unsigned long long epoch = __hip_atomic_load((gu64*)ws, RLX_AGENT);
  asm volatile("" ::: "memory");
#define DMA_WO() dma16(lds0 + LDS_MISC + 1024 * w, (lane >> 2) * XROW + 16 * (lane & 3), rsWo, (28 * (4 * tt + (pi >> 3)) + 16 * ((pi >> 2) & 1)) * XROW + 224 * (gp >> 1) + 64 * (pi & 3))

  bf16x8 xf[28], wk[2], wv[2];
  f32x16 accK = {}, accV = {};
  const unsigned xr = (32 * tt + r32) * 128 + ((hh ^ ((r32 >> 1) & 7)) * 16);
  const unsigned trl = (8 * hh + ((lane & 15) >> 2)) * 128 + ((hs ^ (lane >> 3)) & 1) * 64 + ((lane >> 4) & 1) * 32 + (lane & 3) * 8;
  lptr ldh = lds + 2 * SLOT; PIN(ldh);
#define KSL(c) ((((c) & 2) ? ldh : lds) + ((c) & 1) * SLOT)
  const bool gl = (lane & 19) == 19;
  const unsigned kdl = gl ? 8u : (unsigned)SLOT, kwrap = gl ? 8u : (unsigned)(-3 * SLOT);
  lptr ak = lds + (gl ? LDS_GW + (trl & ~127u) : 16384 + trl);
#define KV_RD(n) do { const lptr s_ = KSL((n) >> 2); xf[n] = ldsr(s_ + (xr ^ (32 * ((n) & 3)))); \
    if ((n) && !((n) & 3)) { ak += (n) == 16 ? kwrap : kdl; PIN(ak); } \
    wk[(n) & 1] = trr2(ak + ((n) & 3) * 2048, 512); wv[(n) & 1] = trr2(s_ + 24576 + trl + ((n) & 3) * 2048, 512); } while (0)
#define KV_STEP(n, MEM) do { SBAR(); if ((n) < 27) KV_RD((n) + 1); SBAR(); \
    accK = MFMA(wk[(n) & 1], xf[n], accK); accV = MFMA(xf[n], wv[(n) & 1], accV); SBAR(); MEM; SBAR(); } while (0)
  asm volatile("s_waitcnt vmcnt(6)" : "+v"(gw) :: "memory");
  *(LAS unsigned short*)(lds + LDS_GW + lane * 128 + w * 8) = (unsigned short)gw;
  WAIT_BAR(6); STAMP(1); ST1(1); KV_RD(0);
  KV_STEP(0, DMA_KV(2, 0); DMA_KV(2, 1)); KV_STEP(1, DMA_KV(2, 2)); KV_STEP(2, DMA_KV(2, 3)); WAIT_BAR(5); ST1(2);
  KV_STEP(3, DMA_KV(3, 0)); KV_STEP(4, DMA_KV(3, 1)); KV_STEP(5, DMA_KV(3, 2)); KV_STEP(6, DMA_KV(3, 3)); WAIT_BAR(4); ST1(3);
  KV_STEP(7, DMA_KV(4, 0)); KV_STEP(8, DMA_KV(4, 1)); KV_STEP(9, DMA_KV(4, 2)); KV_STEP(10, DMA_KV(4, 3)); WAIT_BAR(4); ST1(4);
  KV_STEP(11, DMA_KV(5, 0)); KV_STEP(12, DMA_KV(5, 1)); KV_STEP(13, DMA_KV(5, 2)); KV_STEP(14, DMA_KV(5, 3)); WAIT_BAR(4); ST1(5);
  KV_STEP(15, DMA_KV(6, 0)); KV_STEP(16, DMA_KV(6, 1)); KV_STEP(17, DMA_KV(6, 2)); KV_STEP(18, DMA_KV(6, 3)); WAIT_BAR(4); ST1(6);
  KV_STEP(19, DMA_QA(0)); KV_STEP(20, (void)0); KV_STEP(21, DMA_QA(1)); KV_STEP(22, (void)0); WAIT_BAR(2); ST1(7);
  KV_STEP(23, DMA_QA(2)); KV_STEP(24, (void)0); KV_STEP(25, DMA_QA(3)); KV_STEP(26, (void)0); KV_STEP(27, (void)0);
  STAMP(2); ST1(8);
  bf16x8 k0 = pack8(accK, 0), k1 = pack8(accK, 1), v0 = pack8(accV, 0), v1 = pack8(accV, 1);
  { u32x4 t = __builtin_bit_cast(u32x4, k1); t[2] = hh ? 0x00003f80u : t[2]; t[3] = hh ? 0u : t[3]; k1 = __builtin_bit_cast(bf16x8, t);
    u32x4 a = __builtin_bit_cast(u32x4, v0), b = __builtin_bit_cast(u32x4, v1); const unsigned pv = r32 == 28 ? 0x3f803f80u : 0u;
#pragma unroll
    for (int i = 0; i < 4; ++i) { a[i] = r32 >= 28 ? pv : a[i]; b[i] = r32 >= 28 ? pv : b[i]; }
    v0 = __builtin_bit_cast(bf16x8, a); v1 = __builtin_bit_cast(bf16x8, b); }
  { const unsigned kvo = gbase + j * SLOT + w * 4096;
    __builtin_amdgcn_raw_buffer_store_b128(__builtin_bit_cast(u32x4, k0), rsWS, lane * 16, kvo, 16);
    __builtin_amdgcn_raw_buffer_store_b128(__builtin_bit_cast(u32x4, k1), rsWS, lane * 16 + 1024, kvo, 16);
    __builtin_amdgcn_raw_buffer_store_b128(__builtin_bit_cast(u32x4, v0), rsWS, lane * 16 + 2048, kvo, 16);
    __builtin_amdgcn_raw_buffer_store_b128(__builtin_bit_cast(u32x4, v1), rsWS, lane * 16 + 3072, kvo, 16);
    const lptr own = lds + SLOT + w * 4096 + lane * 16;
    *(LAS bf16x8*)own = k0; *(LAS bf16x8*)(own + 1024) = k1; *(LAS bf16x8*)(own + 2048) = v0; *(LAS bf16x8*)(own + 3072) = v1; }
  float gs;
  { auto r = __builtin_amdgcn_permlane32_swap(__float_as_uint(accK[12]), __float_as_uint(accK[12]), false, false);
    gs = 1.0f / (1.0f + __expf(-__uint_as_float(r[1]))); gs = br ? 1.0f - gs : gs; }
  f32x16 accQ = {};
  WAIT_BAR(4); ST1(9);
#pragma unroll
  for (int ks = 0; ks < FLAGAT; ++ks) accQ = MFMA(trr2(ldh + SLOT + trl + ks * 2048, 512), xf[ks], accQ);
  PIN(accQ);
  asm volatile("s_waitcnt vmcnt(0)" ::: "memory");
  PIN(epoch); epoch += 1;
  if (lane == 0) __hip_atomic_store((gu64*)(ws + WS_FLAG + (gp * 8 + w) * 1024 + pi * 32), epoch, RLX_AGENT);
  DMA_QB(0); DMA_QB(1); DMA_QB(2); DMA_WO();
  ST1(10);
#pragma unroll
  for (int ks = FLAGAT; ks < 16; ++ks) accQ = MFMA(trr2(ldh + SLOT + trl + ks * 2048, 512), xf[ks], accQ);
  PIN(accQ);
  gu64* fp = (gu64*)(ws + WS_FLAG + (gp * 8 + w) * 1024 + ((grp & 1) * 16 + (lane & 15)) * 32);
  STAMP(3);
  WAIT_BAR(1);
  unsigned long long kflag;
#define POLL() asm volatile("global_load_dwordx2 %0, %1, off sc1" : "=v"(kflag) : "v"(fp) : "memory")
  POLL();
#pragma unroll
  for (int ks = 0; ks < 12; ++ks) accQ = MFMA(trr2(lds + trl + ks * 2048, 512), xf[16 + ks], accQ);
#pragma unroll
  for (int i = 0; i < 16; ++i) accQ[i] *= QSCALE;
  bf16x8 q0 = pack8(accQ, 0), q1 = pack8(accQ, 1);
  { u32x4 t = __builtin_bit_cast(u32x4, q1); t[2] = hh ? 0u : t[2]; t[3] = hh ? 0u : t[3]; q1 = __builtin_bit_cast(bf16x8, t); }
  const bf16x8 q1z = q1;
  const f32x16 zero = {};
  {
    f32x16 s = MFMA(k0, q0, zero); s = MFMA(k1, q1, s);
    const float m = rowmax16(s);
    u32x4 t = __builtin_bit_cast(u32x4, q1); t[2] = hh ? (cvtpk(-m, 0.f) & 0xffffu) : t[2]; q1 = __builtin_bit_cast(bf16x8, t); }
  PIN(q1);
  STAMP(4);

  asm volatile("s_waitcnt vmcnt(0)" ::: "memory");
  await_flags(kflag, fp, epoch, (j < 3 ? 15ull : 7ull) & ~(1ull << j));
  STAMP(5); ST2(1);
  bf16x8 wot;
  { const lptr wp = lds + LDS_MISC + w * 1024 + (4 * hh + ((lane & 15) >> 2)) * 64 + ((lane >> 4) & 1) * 32 + (lane & 3) * 8; wot = trr2(wp, 512); }
  const unsigned kvl = lane * 16, kvd = lds0 + w * 4096, kvs = gbase + w * 4096;
#define DMA_P(s, p) dma16_dev(kvd + (((s) + 1) & 3) * SLOT + (p) * 1024, kvl, rsWS, kvs + ((s) - 1 + ((s) - 1 >= j)) * SLOT + (p) * 1024)
  DMA_P(1, 0); DMA_P(1, 1); DMA_P(1, 2); DMA_P(1, 3); DMA_P(2, 0); DMA_P(2, 1); DMA_P(2, 2); DMA_P(2, 3);
  bf16x8 wo[32];
#define WO_LD(ks) do { wo[ks] = __builtin_bit_cast(bf16x8, __builtin_amdgcn_raw_buffer_load_b128(rsWS, kvl, wbase + (ks) * 1024, WOAUX)); asm volatile("" ::: "memory"); } while (0)

  f32x16 o = {}, sa, sb;
  u32x4 p0, p1 = {0u, 0u, 0u, 0u};
  const lptr fb = lds + hs * 16384 + lane * 16;
  lptr fbh = fb + 2 * SLOT; PIN(fbh);
#define SL(s) ((((s) + 1) & 2) ? fbh + (((s) + 1) & 1) * SLOT : fb + (((s) + 1) & 1) * SLOT)
  k0 = ldsr(SL(0)); k1 = ldsr(SL(0) + 1024);
  sa = MFMA(k0, q0, zero); sa = MFMA(k1, q1, sa);
  k0 = ldsr(SL(0) + 4096); k1 = ldsr(SL(0) + 4096 + 1024); v0 = ldsr(SL(0) + 2048); v1 = v0;
#define EX(S, i) do { S[i] = __builtin_amdgcn_exp2f(S[i]); } while (0)
#define STEP(S, T, KP, V1P, V0P, PRIO, MEM0, MEM1) do { \
    SBAR(); SETPRIO(PRIO); EX(S, 0); EX(S, 1); PIN(S); SBAR(); \
    T = MFMA(k0, q0, zero); k0 = ldsr(KP); SBAR(); \
    EX(S, 2); p0[0] = cvtpk(S[0], S[1]); EX(S, 3); EX(S, 4); p0[1] = cvtpk(S[2], S[3]); EX(S, 5); PIN(S); PIN(p0); SBAR(); \
    T = MFMA(k1, q1, T); k1 = ldsr((KP) + 1024); SBAR(); MEM0; SBAR(); \
    EX(S, 6); p0[2] = cvtpk(S[4], S[5]); EX(S, 7); EX(S, 8); p0[3] = cvtpk(S[6], S[7]); EX(S, 9); PIN(S); PIN(p0); SBAR(); \
    o = MFMA(v1, __builtin_bit_cast(bf16x8, p1), o); v1 = ldsr(V1P); SBAR(); \
    EX(S, 10); p1[0] = cvtpk(S[8], S[9]); EX(S, 11); EX(S, 12); p1[1] = cvtpk(S[10], S[11]); EX(S, 13); PIN(S); PIN(p1); SBAR(); \
    o = MFMA(v0, __builtin_bit_cast(bf16x8, p0), o); v0 = ldsr(V0P); SBAR(); MEM1; SBAR(); \
    EX(S, 14); p1[2] = cvtpk(S[12], S[13]); EX(S, 15); p1[3] = cvtpk(S[14], S[15]); PIN(p1); SBAR(); } while (0)
  const u32x4 sent = {~0u, ~0u, ~0u, ~0u};
#define SENT(i) do { __builtin_amdgcn_raw_buffer_store_b128(sent, rsWS, kvl + 1024 * (i), op_own, 16); asm volatile("" ::: "memory"); } while (0)
#define WOT_ST() do { __builtin_amdgcn_raw_buffer_store_b128(__builtin_bit_cast(u32x4, wot), rsWS, kvl, WS_GRP + (gp << 21) + GRP_WOT + w * 32768 + pi * 1024, 16); asm volatile("" ::: "memory"); } while (0)
#define STAGE(s) do { \
    STEP(sa, sb, SL(s) + 2 * 4096, SL(s) + 3072, SL(s) + 4096 + 2048, 1, if ((s) == 0) SENT(0); else if ((s) <= 13) DMA_P((s) + 2, 2), \
         if ((s) >= 5 && (s) <= 14) WO_LD(3 * (s) - 13); else if ((s) == 0) WOT_ST()); \
    STEP(sb, sa, SL(s) + 3 * 4096, SL(s) + 4096 + 3072, SL(s) + 2 * 4096 + 2048, 0, if ((s) == 0) SENT(1); else if ((s) <= 13) DMA_P((s) + 2, 3), \
         if ((s) >= 5 && (s) <= 14) WO_LD(3 * (s) - 12); else if ((s) == 0 || (s) == 3) POLL()); \
    asm volatile("s_waitcnt vmcnt(%0)\n\ts_barrier" ::"n"((s) == 0 ? 8 : (s) == 1 ? 4 : (s) <= 3 ? 5 : (s) == 4 ? 4 : (s) <= 13 ? 8 : (s) == 14 ? 4 : 63) : "memory"); \
    if ((s) < 3) ST2(2 + (s)); \
    if ((s) == 1) { await_flags(kflag, fp, epoch, 0xffffull); \
        \
      if (lane == 0) __hip_atomic_store((gu64*)(ws + WS_FLAG2 + (gp * 8 + w) * 1024 + pi * 32), epoch, RLX_AGENT); \
      fp = (gu64*)(ws + WS_FLAG2 + (hh ? (gpw * 8 + w) * 1024 + r32 * 32 : (w * 8 + (lane & 1) * 4 + (ot & 3)) * 1024 + ((lane & 2) * 8 + (ot >> 2)) * 32)); } \
    if ((s) == 4) await_flags(kflag, fp, epoch, ~0ull); \
    STEP(sa, sb, SL((s) + 1), SL(s) + 2 * 4096 + 3072, SL(s) + 3 * 4096 + 2048, 3, if ((s) <= 12) DMA_P((s) + 3, 0), if ((s) >= 4 && (s) <= 14) WO_LD((s) == 4 ? 0 : 3 * (s) - 11)); \
    STEP(sb, sa, SL((s) + 1) + 4096, SL(s) + 3 * 4096 + 3072, SL((s) + 1) + 2048, 2, if ((s) <= 12) DMA_P((s) + 3, 1), if ((s) == 4) WO_LD(1)); } while (0)
  STAGE(0); STAGE(1); STAGE(2); STAGE(3); STAGE(4); STAGE(5); STAGE(6); STAGE(7);
  STAGE(8); STAGE(9); STAGE(10); STAGE(11); STAGE(12); STAGE(13); STAGE(14); STAGE(15);
  SETPRIO(0);
  o = MFMA(v1, __builtin_bit_cast(bf16x8, p1), o); PIN(o);
  STAMP(6); ST2(5);

  float l;
  { auto r = __builtin_amdgcn_permlane32_swap(__float_as_uint(o[12]), __float_as_uint(o[12]), false, false); l = __uint_as_float(r[1]); }
  { float amax = l;
#pragma unroll
    for (int i = 0; i < 16; ++i) amax = fmaxf(amax, fabsf(o[i]));
    bool bad = !(amax < 1e30f) || !(l > 1e-30f);
#pragma unroll
    for (int i = 0; i < 16; ++i) bad |= (o[i] != o[i]);
    if (__builtin_expect(__any(bad), 0)) {
      float m = -INFINITY; o = zero;
      for (int t = 0; t < 64; ++t) {
        const unsigned ko = gbase + (t >> 2) * SLOT + (hs * 4 + (t & 3)) * 4096;
        const bf16x8 a0 = __builtin_bit_cast(bf16x8, __builtin_amdgcn_raw_buffer_load_b128(rsWS, kvl, ko, 16));
        const bf16x8 a1 = __builtin_bit_cast(bf16x8, __builtin_amdgcn_raw_buffer_load_b128(rsWS, kvl + 1024, ko, 16));
        const bf16x8 b0 = __builtin_bit_cast(bf16x8, __builtin_amdgcn_raw_buffer_load_b128(rsWS, kvl + 2048, ko, 16));
        const bf16x8 b1 = __builtin_bit_cast(bf16x8, __builtin_amdgcn_raw_buffer_load_b128(rsWS, kvl + 3072, ko, 16));
        f32x16 s = MFMA(a0, q0, zero); s = MFMA(a1, q1z, s);
        const float mn = fmaxf(m, rowmax16(s)), al = __builtin_amdgcn_exp2f(m - mn); m = mn;
#pragma unroll
        for (int i = 0; i < 16; ++i) { o[i] *= al; s[i] = __builtin_amdgcn_exp2f(s[i] - mn); }
        o = MFMA(b0, pack8(s, 0), o); o = MFMA(b1, pack8(s, 1), o);
      }
      auto r = __builtin_amdgcn_permlane32_swap(__float_as_uint(o[12]), __float_as_uint(o[12]), false, false); l = __uint_as_float(r[1]);
    } }
  { const float inv = gs / l;
#pragma unroll
    for (int i = 0; i < 16; ++i) o[i] *= inv;
    u32x4 a = __builtin_bit_cast(u32x4, pack8(o, 0)), b = __builtin_bit_cast(u32x4, pack8(o, 1));
    b[2] = hh ? 0u : b[2]; b[3] = hh ? 0u : b[3];
#pragma unroll
    for (int i = 0; i < 4; ++i) { a[i] = min(a[i], 0xfffffffeu); b[i] = min(b[i], 0xfffffffeu); }
    __builtin_amdgcn_raw_buffer_store_b128(a, rsWS, lane * 16, op_own, 16); __builtin_amdgcn_raw_buffer_store_b128(b, rsWS, lane * 16 + 1024, op_own, 16); }
  __builtin_amdgcn_s_barrier();
  STAMP(7); ST2(6);

  f32x16 y[4] = {};
  const lptr rb = lds + SLOT + lane * 16;
  asm volatile("" ::: "memory"); ST2(7);
  { const unsigned po = WS_OP + ot * 65536 + w * 8192; u32x4 pl[8];
#pragma unroll
    for (int i = 0; i < 8; ++i) pl[i] = __builtin_amdgcn_raw_buffer_load_b128(rsWS, kvl, po + 1024 * i, 16);
#pragma unroll
    for (int hf = 0; hf < 2; ++hf) {
      for (unsigned spins = 0; spins < SPIN_CAP; ++spins) {
        unsigned mx = 0;
#pragma unroll
        for (int i = 4 * hf; i < 4 * hf + 4; ++i) mx = max(max(mx, max(pl[i][0], pl[i][1])), max(pl[i][2], pl[i][3]));
        if (!__any(mx == ~0u)) break;
        __builtin_amdgcn_s_sleep(4);
        asm volatile("" ::: "memory");
#pragma unroll
        for (int i = 4 * hf; i < 4 * hf + 4; ++i) pl[i] = __builtin_amdgcn_raw_buffer_load_b128(rsWS, kvl, po + 1024 * i, 16);
      }
      if (hf == 0) {
        ST2(8);
#pragma unroll
        for (int i = 0; i < 4; ++i)
#pragma unroll
          for (int nt = 0; nt < 4; ++nt) y[nt] = MFMA(wo[4 * i + nt], __builtin_bit_cast(bf16x8, pl[i]), y[nt]);
        PIN(y[0]); PIN(y[1]); PIN(y[2]); PIN(y[3]);
      } else {
#pragma unroll
        for (int nt = 0; nt < 5; ++nt) {
          SBAR();
          if (nt < 4)
#pragma unroll
            for (int i = 4; i < 8; ++i) y[nt] = MFMA(wo[4 * i + nt], __builtin_bit_cast(bf16x8, pl[i]), y[nt]);
          SBAR();
          if (nt > 0)
#pragma unroll
            for (int u = 4 * nt - 4; u < min(4 * nt, 14); ++u)
              *(LAS f32x4*)(rb + w * 14336 + u * 1024) = (f32x4){y[u >> 2][4 * (u & 3)], y[u >> 2][4 * (u & 3) + 1], y[u >> 2][4 * (u & 3) + 2], y[u >> 2][4 * (u & 3) + 3]};
        }
      }
    } }
  STAMP(8); ST2(9);
  {
    asm volatile("s_waitcnt lgkmcnt(0)\n\ts_barrier" ::: "memory");
    STAMP(9);
    if (bid == 0 && tid == 0) __hip_atomic_store((gu64*)ws, epoch, RLX_AGENT);
    if (w != 7) {
      unsigned r[4];
#pragma unroll
      for (int u = 0; u < 2; ++u) { f32x4 t[8];
#pragma unroll
        for (int k = 0; k < 8; ++k) t[k] = *(const LAS f32x4*)(rb + k * 14336 + (2 * w + u) * 1024);
        const f32x4 a = (t[0] + t[1]) + (t[2] + t[3]), b = (t[4] + t[5]) + (t[6] + t[7]), m = a + b;
        r[2 * u] = cvtpk(m[0], m[1]); r[2 * u + 1] = cvtpk(m[2], m[3]); }
      auto e0 = __builtin_amdgcn_permlane32_swap(r[0], r[2], false, false), e1 = __builtin_amdgcn_permlane32_swap(r[1], r[3], false, false);
      *(u32x4*)(out + (size_t)(32 * ot + r32) * XROW + (112 * cq + 16 * w + 8 * hh) * 2) = (u32x4){e0[0], e1[0], e0[1], e1[1]};
    } }
  STAMP(10);
  if (bid == 1 && w == 0) { const unsigned long long e_ = hold0 + 6000 + (xb - xa) / 1; for (int n_ = 0; n_ < 400000 && __builtin_amdgcn_s_memrealtime() < e_; ++n_) __builtin_amdgcn_s_sleep(1); }
}

extern "C" void kernel_launch(void* const* d_in, const int* in_sizes, int n_in, void* d_out, int out_size, void* d_ws, size_t ws_size,
                              hipStream_t stream) {
  gated_mha_fused<<<256, 512, 0, stream>>>((const char*)d_in[0], (const char*)d_in[1], (const char*)d_in[2], (const char*)d_in[4],
                                           (const char*)d_in[5], (const char*)d_in[6], (char*)d_out, (char*)d_ws);
}
```

```cpp
#include <hip/hip_runtime.h>
#include <hip/hip_bf16.h>
#include <stdint.h>

typedef short    bf16x8 __attribute__((ext_vector_type(8)));
typedef short    s16x4  __attribute__((ext_vector_type(4)));
typedef float    f32x16 __attribute__((ext_vector_type(16)));
typedef float    f32x2  __attribute__((ext_vector_type(2)));
typedef float    f32x4  __attribute__((ext_vector_type(4)));
typedef __bf16   bf16x2 __attribute__((ext_vector_type(2)));
typedef unsigned u32x4  __attribute__((ext_vector_type(4)));
typedef unsigned u32x2  __attribute__((ext_vector_type(2)));
typedef __amdgpu_buffer_rsrc_t rsrc_t;
#define DI __device__ __forceinline__
#define LAS __attribute__((address_space(3)))
#define GAS __attribute__((address_space(1)))
typedef LAS char* lptr;
typedef GAS unsigned long long gu64;

#define KVSC "sc1"
#define WOAUX 16
#define XBYTES (SEQ * XROW)
#define WBYTES (DM * WROW)
#define SETPRIO(p) __builtin_amdgcn_s_setprio(p)
#define MFMA(a, b, c) __builtin_amdgcn_mfma_f32_32x32x16_bf16((a), (b), (c), 0, 0, 0)
#define SBAR() __builtin_amdgcn_sched_barrier(0)
#define PIN(x) asm volatile("" : "+v"(x))
#define WAIT_BAR(N) asm volatile("s_waitcnt vmcnt(" #N ") lgkmcnt(0)\n\ts_barrier" ::: "memory")
#define RLX_AGENT __ATOMIC_RELAXED, __HIP_MEMORY_SCOPE_AGENT

constexpr int SEQ = 2048, DM = 448, XROW = DM * 2, WROW = 3 * DM * 2;
constexpr unsigned WS_FLAG = 4096, WS_FLAG2 = WS_FLAG + 65536, WS_GRP = 2u << 20, GRP_WOT = 512u << 10, GRP_PAD = 768u << 10, WS_OP = WS_GRP + (16u << 20), WS_END = WS_OP + (4u << 20);
constexpr unsigned WS_DBG = WS_END;
constexpr int SLOT = 32768, LDS_MISC = 4 * SLOT, LDS_GW = LDS_MISC + 8192, LDS_JUNK = LDS_MISC + 16384, LDS_BYTES = LDS_JUNK + 256;
#define OUTAUX 17
#define FLAGAT 6
constexpr unsigned SPIN_CAP = 1u << 18;
constexpr float QSCALE = 0.18898223650461362f * 1.4426950408889634f;

DI unsigned cvtpk(float lo, float hi) { f32x2 v = {lo, hi}; return __builtin_bit_cast(unsigned, __builtin_convertvector(v, bf16x2)); }
DI bf16x8 pack8(const f32x16& x, int s) {
  u32x4 p = {cvtpk(x[8 * s], x[8 * s + 1]), cvtpk(x[8 * s + 2], x[8 * s + 3]), cvtpk(x[8 * s + 4], x[8 * s + 5]), cvtpk(x[8 * s + 6], x[8 * s + 7])};
  return __builtin_bit_cast(bf16x8, p);
}
DI void dma16(unsigned lds_dst, unsigned voff, rsrc_t rs, unsigned soff) {
  asm volatile("s_mov_b32 m0, %0\n\ts_nop 0\n\tbuffer_load_dwordx4 %1, %2, %3 offen lds" ::"s"(lds_dst), "v"(voff), "s"(rs), "s"(soff) : "memory");
}
DI void dma16_dev(unsigned lds_dst, unsigned voff, rsrc_t rs, unsigned soff) {
  asm volatile("s_mov_b32 m0, %0\n\ts_nop 0\n\tbuffer_load_dwordx4 %1, %2, %3 offen " KVSC " lds" ::"s"(lds_dst), "v"(voff), "s"(rs), "s"(soff) : "memory");
}
DI void await_flags(unsigned long long& f, gu64* p, unsigned long long epoch, unsigned long long need) {
  unsigned n;
  asm volatile("s_mov_b32 %1, 0\n"
               "1:\n\tv_cmp_ne_u64 vcc, %0, %3\n\ts_and_b64 vcc, vcc, %4\n\ts_cbranch_scc0 2f\n\t"
               "s_sleep 1\n\tglobal_load_dwordx2 %0, %2, off sc1\n\ts_add_u32 %1, %1, 1\n\ts_cmp_lt_u32 %1, %5\n\ts_waitcnt vmcnt(0)\n\ts_cbranch_scc1 1b\n"
               "2:" : "+v"(f), "=&s"(n) : "v"(p), "v"(epoch), "s"(need), "s"(SPIN_CAP) : "vcc", "scc", "memory");
}
DI bf16x8 ldsr(lptr p) { return *(const LAS bf16x8*)p; }
DI s16x4 trr(lptr p) { return __builtin_amdgcn_ds_read_tr16_b64_v4i16((LAS s16x4*)p); }
DI bf16x8 trr2(lptr p, int second) { s16x4 lo = trr(p), hi = trr(p + second); return __builtin_shufflevector(lo, hi, 0, 1, 2, 3, 4, 5, 6, 7); }
DI float swap_max(float m) { auto r = __builtin_amdgcn_permlane32_swap(__float_as_uint(m), __float_as_uint(m), false, false); return fmaxf(__uint_as_float(r[0]), __uint_as_float(r[1])); }
DI float rowmax16(const f32x16& s) {
  float a = fmaxf(fmaxf(s[0], s[1]), s[2]), b = fmaxf(fmaxf(s[3], s[4]), s[5]);
  a = fmaxf(fmaxf(a, s[6]), s[7]); b = fmaxf(fmaxf(b, s[8]), s[9]); a = fmaxf(fmaxf(a, s[10]), s[11]); b = fmaxf(fmaxf(b, s[12]), s[13]);
  a = fmaxf(fmaxf(a, s[14]), s[15]);
  return swap_max(fmaxf(a, b));
}
DI float dot2(unsigned a, unsigned b, float c) { return __builtin_amdgcn_fdot2_f32_bf16(__builtin_bit_cast(bf16x2, a), __builtin_bit_cast(bf16x2, b), c, false); }
DI float bflo(unsigned u) { return __uint_as_float(u << 16); }
DI float bfhi(unsigned u) { return __uint_as_float(u & 0xffff0000u); }

#define STAMP(i) do { } while (0)
#define ST1(i) do { } while (0)
#define ST2(i) do { } while (0)

__global__ __launch_bounds__(512, 2) void gated_mha_fused(const char* __restrict__ x, const char* __restrict__ wqkvA, const char* __restrict__ woA,
                                                          const char* __restrict__ wqkvB, const char* __restrict__ woB, const char* __restrict__ gate,
                                                          char* __restrict__ out, char* ws) {
  __shared__ __attribute__((aligned(1024))) char lds_raw[LDS_BYTES];
  STAMP(0);
  const lptr lds = (lptr)lds_raw;
  const unsigned lds0 = (unsigned)(uintptr_t)lds;
  const int tid = threadIdx.x, lane = tid & 63, r32 = lane & 31, hh = lane >> 5;
  const int w = __builtin_amdgcn_readfirstlane(tid >> 6), tt = w & 3, hs = w >> 2;
  const int bid = blockIdx.x, grp = (bid & 7) * 2 + ((bid >> 3) & 1), j = bid >> 4, br = grp >> 3, hp = grp & 7;

  const int gp = bid & 7, pi = (grp & 1) * 16 + j;
  const int ot = 8 * gp + (pi >> 2), cq = pi & 3, gpw = 2 * cq + (gp & 1);
  const unsigned gbase = WS_GRP + (grp << 20), wbase = WS_GRP + (gpw << 21) + GRP_WOT + w * 32768;
  const rsrc_t rsX = __builtin_amdgcn_make_buffer_rsrc((void*)x, 0, XBYTES, 0x00020000);
  const rsrc_t rsW = __builtin_amdgcn_make_buffer_rsrc((void*)(br ? wqkvB : wqkvA), 0, WBYTES, 0x00020000);
  const rsrc_t rsWo = __builtin_amdgcn_make_buffer_rsrc((void*)(hs ? woB : woA), 0, DM * XROW, 0x00020000);
  const rsrc_t rsG = __builtin_amdgcn_make_buffer_rsrc((void*)gate, 0, XROW, 0x00020000);
  const rsrc_t rsWS = __builtin_amdgcn_make_buffer_rsrc((void*)ws, 0, WS_END, 0x00020000);

  const unsigned xrow = 16 * w + (lane >> 3);
  const unsigned xoff0 = (128 * j + xrow) * XROW + 16 * ((lane & 7) ^ ((lane >> 4) & 7));
  const unsigned xoff1 = (128 * j + xrow + 8) * XROW + 16 * ((lane & 7) ^ ((4 + (lane >> 4)) & 7));
  const unsigned wl = (lane >> 3) * WROW + (((lane >> 2) ^ (lane >> 4)) & 1) * 56 + 16 * (lane & 3) + hp * 112;
  const unsigned wkv_s = 16 * tt * WROW + (hs + 1) * XROW;
#define DMA_KV(c, i) do { const unsigned sl_ = lds0 + ((c) & 3) * SLOT + 2048 * w + ((i) >> 1) * 1024; \
    if ((i) & 1) dma16(sl_ + 16384, wl, rsW, wkv_s + (64 * (c) + 8 * ((i) >> 1)) * WROW); else dma16(sl_, ((i) >> 1) ? xoff1 : xoff0, rsX, 128 * (c)); } while (0)
#define DMA_KV4(c) do { DMA_KV(c, 0); DMA_KV(c, 1); DMA_KV(c, 2); DMA_KV(c, 3); } while (0)
#define DMA_QA(q) dma16(lds0 + 3 * SLOT + 4096 * w + 1024 * (q), wl, rsW, (32 * w + 8 * (q)) * WROW)
#define DMA_QB(q) dma16(lds0 + 3072 * w + 1024 * (q), wl, rsW, (256 + 24 * w + 8 * (q)) * WROW)
  const unsigned op_own = WS_OP + ((j * 4 + tt) * 32 + 2 * grp + hs) * 2048;
  DMA_KV4(0);
  unsigned gw;
  asm volatile("buffer_load_ushort %0, %1, %2, 0 offen" : "=v"(gw) : "v"(tid * 2), "s"(rsG) : "memory");
  {
    const char* code; unsigned clen;
    asm volatile("s_getpc_b64 %0\n.Lgm_lo:\n\ts_mov_b32 %1, .Lgm_hi-.Lgm_lo" : "=s"(code), "=s"(clen));
    const char* tp = w == 0 ? x + (size_t)min(bid * 56 + lane, 14335) * 128
                   : w < 3 ? (w == 1 ? wqkvA : wqkvB) + (size_t)min(bid * 37 + min(lane, 36), 9407) * 128
                   : w < 5 ? (w == 3 ? woA : woB) + (size_t)min(bid * 13 + min(lane, 12), 3135) * 128
                   : w < 7 ? ws + (w == 5 ? WS_GRP + ((hh ? gpw : gp) << 21) + GRP_PAD : hh ? op_own : WS_OP + ot * 65536)
                   : lane < 2 ? out + (size_t)(32 * ot + 31 * lane) * XROW : code + min(((pi & 3) * 62 + lane - 2) * 128u, clen);
    asm volatile("s_mov_b32 m0, %0\n\ts_nop 0\n\tglobal_load_lds_dword %1, off sc1" ::"s"(lds0 + LDS_JUNK), "v"(tp) : "memory"); }
  DMA_KV4(1);
  unsigned long long epoch = __hip_atomic_load((gu64*)ws, RLX_AGENT);
  asm volatile("" ::: "memory");
#define DMA_WO() dma16(lds0 + LDS_MISC + 1024 * w, (lane >> 2) * XROW + 16 * (lane & 3), rsWo, (28 * (4 * tt + (pi >> 3)) + 16 * ((pi >> 2) & 1)) * XROW + 224 * (gp >> 1) + 64 * (pi & 3))

  bf16x8 xf[28], wk[2], wv[2];
  f32x16 accK = {}, accV = {};
  const unsigned xr = (32 * tt + r32) * 128 + ((hh ^ ((r32 >> 1) & 7)) * 16);
  const unsigned trl = (8 * hh + ((lane & 15) >> 2)) * 128 + ((hs ^ (lane >> 3)) & 1) * 64 + ((lane >> 4) & 1) * 32 + (lane & 3) * 8;
  lptr ldh = lds + 2 * SLOT; PIN(ldh);
#define KSL(c) ((((c) & 2) ? ldh : lds) + ((c) & 1) * SLOT)
  const bool gl = (lane & 19) == 19;
  const unsigned kdl = gl ? 8u : (unsigned)SLOT, kwrap = gl ? 8u : (unsigned)(-3 * SLOT);
  lptr ak = lds + (gl ? LDS_GW + (trl & ~127u) : 16384 + trl);
#define KV_RD(n) do { const lptr s_ = KSL((n) >> 2); xf[n] = ldsr(s_ + (xr ^ (32 * ((n) & 3)))); \
    if ((n) && !((n) & 3)) { ak += (n) == 16 ? kwrap : kdl; PIN(ak); } \
    wk[(n) & 1] = trr2(ak + ((n) & 3) * 2048, 512); wv[(n) & 1] = trr2(s_ + 24576 + trl + ((n) & 3) * 2048, 512); } while (0)
#define KV_STEP(n, MEM) do { SBAR(); if ((n) < 27) KV_RD((n) + 1); SBAR(); \
    accK = MFMA(wk[(n) & 1], xf[n], accK); accV = MFMA(xf[n], wv[(n) & 1], accV); SBAR(); MEM; SBAR(); } while (0)
  asm volatile("s_waitcnt vmcnt(6)" : "+v"(gw) :: "memory");
  *(LAS unsigned short*)(lds + LDS_GW + lane * 128 + w * 8) = (unsigned short)gw;
  WAIT_BAR(6); STAMP(1); ST1(1); KV_RD(0);
  KV_STEP(0, DMA_KV(2, 0); DMA_KV(2, 1)); KV_STEP(1, DMA_KV(2, 2)); KV_STEP(2, DMA_KV(2, 3)); WAIT_BAR(5); ST1(2);
  KV_STEP(3, DMA_KV(3, 0)); KV_STEP(4, DMA_KV(3, 1)); KV_STEP(5, DMA_KV(3, 2)); KV_STEP(6, DMA_KV(3, 3)); WAIT_BAR(4); ST1(3);
  KV_STEP(7, DMA_KV(4, 0)); KV_STEP(8, DMA_KV(4, 1)); KV_STEP(9, DMA_KV(4, 2)); KV_STEP(10, DMA_KV(4, 3)); WAIT_BAR(4); ST1(4);
  KV_STEP(11, DMA_KV(5, 0)); KV_STEP(12, DMA_KV(5, 1)); KV_STEP(13, DMA_KV(5, 2)); KV_STEP(14, DMA_KV(5, 3)); WAIT_BAR(4); ST1(5);
  KV_STEP(15, DMA_KV(6, 0)); KV_STEP(16, DMA_KV(6, 1)); KV_STEP(17, DMA_KV(6, 2)); KV_STEP(18, DMA_KV(6, 3)); WAIT_BAR(4); ST1(6);
  KV_STEP(19, DMA_QA(0)); KV_STEP(20, (void)0); KV_STEP(21, DMA_QA(1)); KV_STEP(22, (void)0); WAIT_BAR(2); ST1(7);
  KV_STEP(23, DMA_QA(2)); KV_STEP(24, (void)0); KV_STEP(25, DMA_QA(3)); KV_STEP(26, (void)0); KV_STEP(27, (void)0);
  STAMP(2); ST1(8);
  bf16x8 k0 = pack8(accK, 0), k1 = pack8(accK, 1), v0 = pack8(accV, 0), v1 = pack8(accV, 1);
  { u32x4 t = __builtin_bit_cast(u32x4, k1); t[2] = hh ? 0x00003f80u : t[2]; t[3] = hh ? 0u : t[3]; k1 = __builtin_bit_cast(bf16x8, t);
    u32x4 a = __builtin_bit_cast(u32x4, v0), b = __builtin_bit_cast(u32x4, v1); const unsigned pv = r32 == 28 ? 0x3f803f80u : 0u;
#pragma unroll
    for (int i = 0; i < 4; ++i) { a[i] = r32 >= 28 ? pv : a[i]; b[i] = r32 >= 28 ? pv : b[i]; }
    v0 = __builtin_bit_cast(bf16x8, a); v1 = __builtin_bit_cast(bf16x8, b); }
  { const unsigned kvo = gbase + j * SLOT + w * 4096;
    __builtin_amdgcn_raw_buffer_store_b128(__builtin_bit_cast(u32x4, k0), rsWS, lane * 16, kvo, 16);
    __builtin_amdgcn_raw_buffer_store_b128(__builtin_bit_cast(u32x4, k1), rsWS, lane * 16 + 1024, kvo, 16);
    __builtin_amdgcn_raw_buffer_store_b128(__builtin_bit_cast(u32x4, v0), rsWS, lane * 16 + 2048, kvo, 16);
    __builtin_amdgcn_raw_buffer_store_b128(__builtin_bit_cast(u32x4, v1), rsWS, lane * 16 + 3072, kvo, 16);
    const lptr own = lds + SLOT + w * 4096 + lane * 16;
    *(LAS bf16x8*)own = k0; *(LAS bf16x8*)(own + 1024) = k1; *(LAS bf16x8*)(own + 2048) = v0; *(LAS bf16x8*)(own + 3072) = v1; }
  float gs;
  { auto r = __builtin_amdgcn_permlane32_swap(__float_as_uint(accK[12]), __float_as_uint(accK[12]), false, false);
    gs = 1.0f / (1.0f + __expf(-__uint_as_float(r[1]))); gs = br ? 1.0f - gs : gs; }
  f32x16 accQ = {};
  WAIT_BAR(4); ST1(9);
#pragma unroll
  for (int ks = 0; ks < FLAGAT; ++ks) accQ = MFMA(trr2(ldh + SLOT + trl + ks * 2048, 512), xf[ks], accQ);
  PIN(accQ);
  asm volatile("s_waitcnt vmcnt(0)" ::: "memory");
  PIN(epoch); epoch += 1;
  if (lane == 0) __hip_atomic_store((gu64*)(ws + WS_FLAG + (gp * 8 + w) * 1024 + pi * 32), epoch, RLX_AGENT);
  DMA_QB(0); DMA_QB(1); DMA_QB(2); DMA_WO();
  ST1(10);
#pragma unroll
  for (int ks = FLAGAT; ks < 16; ++ks) accQ = MFMA(trr2(ldh + SLOT + trl + ks * 2048, 512), xf[ks], accQ);
  PIN(accQ);
  gu64* fp = (gu64*)(ws + WS_FLAG + (gp * 8 + w) * 1024 + ((grp & 1) * 16 + (lane & 15)) * 32);
  STAMP(3);
  WAIT_BAR(1);
  unsigned long long kflag;
#define POLL() asm volatile("global_load_dwordx2 %0, %1, off sc1" : "=v"(kflag) : "v"(fp) : "memory")
  POLL();
#pragma unroll
  for (int ks = 0; ks < 12; ++ks) accQ = MFMA(trr2(lds + trl + ks * 2048, 512), xf[16 + ks], accQ);
#pragma unroll
  for (int i = 0; i < 16; ++i) accQ[i] *= QSCALE;
  bf16x8 q0 = pack8(accQ, 0), q1 = pack8(accQ, 1);
  { u32x4 t = __builtin_bit_cast(u32x4, q1); t[2] = hh ? 0u : t[2]; t[3] = hh ? 0u : t[3]; q1 = __builtin_bit_cast(bf16x8, t); }
  const bf16x8 q1z = q1;
  const f32x16 zero = {};
  {
    f32x16 s = MFMA(k0, q0, zero); s = MFMA(k1, q1, s);
    const float m = rowmax16(s);
    u32x4 t = __builtin_bit_cast(u32x4, q1); t[2] = hh ? (cvtpk(-m, 0.f) & 0xffffu) : t[2]; q1 = __builtin_bit_cast(bf16x8, t); }
  PIN(q1);
  STAMP(4);

  asm volatile("s_waitcnt vmcnt(0)" ::: "memory");
  await_flags(kflag, fp, epoch, (j < 3 ? 15ull : 7ull) & ~(1ull << j));
  STAMP(5); ST2(1);
  bf16x8 wot;
  { const lptr wp = lds + LDS_MISC + w * 1024 + (4 * hh + ((lane & 15) >> 2)) * 64 + ((lane >> 4) & 1) * 32 + (lane & 3) * 8; wot = trr2(wp, 512); }
  const unsigned kvl = lane * 16, kvd = lds0 + w * 4096, kvs = gbase + w * 4096;
#define DMA_P(s, p) dma16_dev(kvd + (((s) + 1) & 3) * SLOT + (p) * 1024, kvl, rsWS, kvs + ((s) - 1 + ((s) - 1 >= j)) * SLOT + (p) * 1024)
  DMA_P(1, 0); DMA_P(1, 1); DMA_P(1, 2); DMA_P(1, 3); DMA_P(2, 0); DMA_P(2, 1); DMA_P(2, 2); DMA_P(2, 3);
  bf16x8 wo[32];
#define WO_LD(ks) do { wo[ks] = __builtin_bit_cast(bf16x8, __builtin_amdgcn_raw_buffer_load_b128(rsWS, kvl, wbase + (ks) * 1024, WOAUX)); asm volatile("" ::: "memory"); } while (0)

  f32x16 o = {}, sa, sb;
  u32x4 p0, p1 = {0u, 0u, 0u, 0u};
  const lptr fb = lds + hs * 16384 + lane * 16;
  lptr fbh = fb + 2 * SLOT; PIN(fbh);
#define SL(s) ((((s) + 1) & 2) ? fbh + (((s) + 1) & 1) * SLOT : fb + (((s) + 1) & 1) * SLOT)
  k0 = ldsr(SL(0)); k1 = ldsr(SL(0) + 1024);
  sa = MFMA(k0, q0, zero); sa = MFMA(k1, q1, sa);
  k0 = ldsr(SL(0) + 4096); k1 = ldsr(SL(0) + 4096 + 1024); v0 = ldsr(SL(0) + 2048); v1 = v0;
#define EX(S, i) do { S[i] = __builtin_amdgcn_exp2f(S[i]); } while (0)
#define STEP(S, T, KP, V1P, V0P, PRIO, MEM0, MEM1) do { \
    SBAR(); SETPRIO(PRIO); EX(S, 0); EX(S, 1); PIN(S); SBAR(); \
    T = MFMA(k0, q0, zero); k0 = ldsr(KP); SBAR(); \
    EX(S, 2); p0[0] = cvtpk(S[0], S[1]); EX(S, 3); EX(S, 4); p0[1] = cvtpk(S[2], S[3]); EX(S, 5); PIN(S); PIN(p0); SBAR(); \
    T = MFMA(k1, q1, T); k1 = ldsr((KP) + 1024); SBAR(); MEM0; SBAR(); \
    EX(S, 6); p0[2] = cvtpk(S[4], S[5]); EX(S, 7); EX(S, 8); p0[3] = cvtpk(S[6], S[7]); EX(S, 9); PIN(S); PIN(p0); SBAR(); \
    o = MFMA(v1, __builtin_bit_cast(bf16x8, p1), o); v1 = ldsr(V1P); SBAR(); \
    EX(S, 10); p1[0] = cvtpk(S[8], S[9]); EX(S, 11); EX(S, 12); p1[1] = cvtpk(S[10], S[11]); EX(S, 13); PIN(S); PIN(p1); SBAR(); \
    o = MFMA(v0, __builtin_bit_cast(bf16x8, p0), o); v0 = ldsr(V0P); SBAR(); MEM1; SBAR(); \
    EX(S, 14); p1[2] = cvtpk(S[12], S[13]); EX(S, 15); p1[3] = cvtpk(S[14], S[15]); PIN(p1); SBAR(); } while (0)
  const u32x4 sent = {~0u, ~0u, ~0u, ~0u};
#define SENT(i) do { __builtin_amdgcn_raw_buffer_store_b128(sent, rsWS, kvl + 1024 * (i), op_own, 16); asm volatile("" ::: "memory"); } while (0)
#define WOT_ST() do { __builtin_amdgcn_raw_buffer_store_b128(__builtin_bit_cast(u32x4, wot), rsWS, kvl, WS_GRP + (gp << 21) + GRP_WOT + w * 32768 + pi * 1024, 16); asm volatile("" ::: "memory"); } while (0)
#define STAGE(s) do { \
    STEP(sa, sb, SL(s) + 2 * 4096, SL(s) + 3072, SL(s) + 4096 + 2048, 1, if ((s) == 0) SENT(0); else if ((s) <= 13) DMA_P((s) + 2, 2), \
         if ((s) >= 5 && (s) <= 14) WO_LD(3 * (s) - 13); else if ((s) == 0) WOT_ST()); \
    STEP(sb, sa, SL(s) + 3 * 4096, SL(s) + 4096 + 3072, SL(s) + 2 * 4096 + 2048, 0, if ((s) == 0) SENT(1); else if ((s) <= 13) DMA_P((s) + 2, 3), \
         if ((s) >= 5 && (s) <= 14) WO_LD(3 * (s) - 12); else if ((s) == 0 || (s) == 3) POLL()); \
    asm volatile("s_waitcnt vmcnt(%0)\n\ts_barrier" ::"n"((s) == 0 ? 8 : (s) == 1 ? 4 : (s) <= 3 ? 5 : (s) == 4 ? 4 : (s) <= 13 ? 8 : (s) == 14 ? 4 : 63) : "memory"); \
    if ((s) < 3) ST2(2 + (s)); \
    if ((s) == 1) { await_flags(kflag, fp, epoch, 0xffffull); \
        \
      if (lane == 0) __hip_atomic_store((gu64*)(ws + WS_FLAG2 + (gp * 8 + w) * 1024 + pi * 32), epoch, RLX_AGENT); \
      fp = (gu64*)(ws + WS_FLAG2 + (hh ? (gpw * 8 + w) * 1024 + r32 * 32 : (w * 8 + (lane & 1) * 4 + (ot & 3)) * 1024 + ((lane & 2) * 8 + (ot >> 2)) * 32)); } \
    if ((s) == 4) await_flags(kflag, fp, epoch, ~0ull); \
    STEP(sa, sb, SL((s) + 1), SL(s) + 2 * 4096 + 3072, SL(s) + 3 * 4096 + 2048, 3, if ((s) <= 12) DMA_P((s) + 3, 0), if ((s) >= 4 && (s) <= 14) WO_LD((s) == 4 ? 0 : 3 * (s) - 11)); \
    STEP(sb, sa, SL((s) + 1) + 4096, SL(s) + 3 * 4096 + 3072, SL((s) + 1) + 2048, 2, if ((s) <= 12) DMA_P((s) + 3, 1), if ((s) == 4) WO_LD(1)); } while (0)
  STAGE(0); STAGE(1); STAGE(2); STAGE(3); STAGE(4); STAGE(5); STAGE(6); STAGE(7);
  STAGE(8); STAGE(9); STAGE(10); STAGE(11); STAGE(12); STAGE(13); STAGE(14); STAGE(15);
  SETPRIO(0);
  o = MFMA(v1, __builtin_bit_cast(bf16x8, p1), o); PIN(o);
  STAMP(6); ST2(5);

  float l;
  { auto r = __builtin_amdgcn_permlane32_swap(__float_as_uint(o[12]), __float_as_uint(o[12]), false, false); l = __uint_as_float(r[1]); }
  { float amax = l;
#pragma unroll
    for (int i = 0; i < 16; ++i) amax = fmaxf(amax, fabsf(o[i]));
    bool bad = !(amax < 1e30f) || !(l > 1e-30f);
#pragma unroll
    for (int i = 0; i < 16; ++i) bad |= (o[i] != o[i]);
    if (__builtin_expect(__any(bad), 0)) {
      float m = -INFINITY; o = zero;
      for (int t = 0; t < 64; ++t) {
        const unsigned ko = gbase + (t >> 2) * SLOT + (hs * 4 + (t & 3)) * 4096;
        const bf16x8 a0 = __builtin_bit_cast(bf16x8, __builtin_amdgcn_raw_buffer_load_b128(rsWS, kvl, ko, 16));
        const bf16x8 a1 = __builtin_bit_cast(bf16x8, __builtin_amdgcn_raw_buffer_load_b128(rsWS, kvl + 1024, ko, 16));
        const bf16x8 b0 = __builtin_bit_cast(bf16x8, __builtin_amdgcn_raw_buffer_load_b128(rsWS, kvl + 2048, ko, 16));
        const bf16x8 b1 = __builtin_bit_cast(bf16x8, __builtin_amdgcn_raw_buffer_load_b128(rsWS, kvl + 3072, ko, 16));
        f32x16 s = MFMA(a0, q0, zero); s = MFMA(a1, q1z, s);
        const float mn = fmaxf(m, rowmax16(s)), al = __builtin_amdgcn_exp2f(m - mn); m = mn;
#pragma unroll
        for (int i = 0; i < 16; ++i) { o[i] *= al; s[i] = __builtin_amdgcn_exp2f(s[i] - mn); }
        o = MFMA(b0, pack8(s, 0), o); o = MFMA(b1, pack8(s, 1), o);
      }
      auto r = __builtin_amdgcn_permlane32_swap(__float_as_uint(o[12]), __float_as_uint(o[12]), false, false); l = __uint_as_float(r[1]);
    } }
  { const float inv = gs / l;
#pragma unroll
    for (int i = 0; i < 16; ++i) o[i] *= inv;
    u32x4 a = __builtin_bit_cast(u32x4, pack8(o, 0)), b = __builtin_bit_cast(u32x4, pack8(o, 1));
    b[2] = hh ? 0u : b[2]; b[3] = hh ? 0u : b[3];
#pragma unroll
    for (int i = 0; i < 4; ++i) { a[i] = min(a[i], 0xfffffffeu); b[i] = min(b[i], 0xfffffffeu); }
    __builtin_amdgcn_raw_buffer_store_b128(a, rsWS, lane * 16, op_own, 16); __builtin_amdgcn_raw_buffer_store_b128(b, rsWS, lane * 16 + 1024, op_own, 16); }
  __builtin_amdgcn_s_barrier();
  STAMP(7); ST2(6);

  f32x16 y[4] = {};
  const lptr rb = lds + SLOT + lane * 16;
  asm volatile("" ::: "memory"); ST2(7);
  { const unsigned po = WS_OP + ot * 65536 + w * 8192; u32x4 pl[8];
#pragma unroll
    for (int i = 0; i < 8; ++i) pl[i] = __builtin_amdgcn_raw_buffer_load_b128(rsWS, kvl, po + 1024 * i, 16);
#pragma unroll
    for (int hf = 0; hf < 2; ++hf) {
      for (unsigned spins = 0; spins < SPIN_CAP; ++spins) {
        unsigned mx = 0;
#pragma unroll
        for (int i = 4 * hf; i < 4 * hf + 4; ++i) mx = max(max(mx, max(pl[i][0], pl[i][1])), max(pl[i][2], pl[i][3]));
        if (!__any(mx == ~0u)) break;
        __builtin_amdgcn_s_sleep(4);
        asm volatile("" ::: "memory");
#pragma unroll
        for (int i = 4 * hf; i < 4 * hf + 4; ++i) pl[i] = __builtin_amdgcn_raw_buffer_load_b128(rsWS, kvl, po + 1024 * i, 16);
      }
      if (hf == 0) {
        ST2(8);
#pragma unroll
        for (int i = 0; i < 4; ++i)
#pragma unroll
          for (int nt = 0; nt < 4; ++nt) y[nt] = MFMA(wo[4 * i + nt], __builtin_bit_cast(bf16x8, pl[i]), y[nt]);
        PIN(y[0]); PIN(y[1]); PIN(y[2]); PIN(y[3]);
      } else {
#pragma unroll
        for (int nt = 0; nt < 5; ++nt) {
          SBAR();
          if (nt < 4)
#pragma unroll
            for (int i = 4; i < 8; ++i) y[nt] = MFMA(wo[4 * i + nt], __builtin_bit_cast(bf16x8, pl[i]), y[nt]);
          SBAR();
          if (nt > 0)
#pragma unroll
            for (int u = 4 * nt - 4; u < min(4 * nt, 14); ++u)
              *(LAS f32x4*)(rb + w * 14336 + u * 1024) = (f32x4){y[u >> 2][4 * (u & 3)], y[u >> 2][4 * (u & 3) + 1], y[u >> 2][4 * (u & 3) + 2], y[u >> 2][4 * (u & 3) + 3]};
        }
      }
    } }
  STAMP(8); ST2(9);
  {
    asm volatile("s_waitcnt lgkmcnt(0)\n\ts_barrier" ::: "memory");
    STAMP(9);
    asm volatile(".Lgm_hi:");
    if (bid == 0 && tid == 0) __hip_atomic_store((gu64*)ws, epoch, RLX_AGENT);
    if (w != 7) {
      unsigned r[4];
#pragma unroll
      for (int u = 0; u < 2; ++u) { f32x4 t[8];
#pragma unroll
        for (int k = 0; k < 8; ++k) t[k] = *(const LAS f32x4*)(rb + k * 14336 + (2 * w + u) * 1024);
        const f32x4 a = (t[0] + t[1]) + (t[2] + t[3]), b = (t[4] + t[5]) + (t[6] + t[7]), m = a + b;
        r[2 * u] = cvtpk(m[0], m[1]); r[2 * u + 1] = cvtpk(m[2], m[3]); }
      auto e0 = __builtin_amdgcn_permlane32_swap(r[0], r[2], false, false), e1 = __builtin_amdgcn_permlane32_swap(r[1], r[3], false, false);
      __builtin_amdgcn_raw_buffer_store_b128((u32x4){e0[0], e1[0], e0[1], e1[1]}, __builtin_amdgcn_make_buffer_rsrc((void*)out, 0, SEQ * XROW, 0x00020000),
                                             r32 * XROW + 16 * hh, 32 * ot * XROW + (112 * cq + 16 * w) * 2, OUTAUX);
    } }
}

extern "C" void kernel_launch(void* const* d_in, const int* in_sizes, int n_in, void* d_out, int out_size, void* d_ws, size_t ws_size,
                              hipStream_t stream) {
  gated_mha_fused<<<256, 512, 0, stream>>>((const char*)d_in[0], (const char*)d_in[1], (const char*)d_in[2], (const char*)d_in[4],
                                           (const char*)d_in[5], (const char*)d_in[6], (char*)d_out, (char*)d_ws);
}
```

```cpp
#include <hip/hip_runtime.h>
#include <hip/hip_bf16.h>
#include <stdint.h>

typedef short    bf16x8 __attribute__((ext_vector_type(8)));
typedef short    s16x4  __attribute__((ext_vector_type(4)));
typedef float    f32x16 __attribute__((ext_vector_type(16)));
typedef float    f32x2  __attribute__((ext_vector_type(2)));
typedef float    f32x4  __attribute__((ext_vector_type(4)));
typedef __bf16   bf16x2 __attribute__((ext_vector_type(2)));
typedef unsigned u32x4  __attribute__((ext_vector_type(4)));
typedef unsigned u32x2  __attribute__((ext_vector_type(2)));
typedef __amdgpu_buffer_rsrc_t rsrc_t;
#define DI __device__ __forceinline__
#define LAS __attribute__((address_space(3)))
#define GAS __attribute__((address_space(1)))
typedef LAS char* lptr;
typedef GAS unsigned long long gu64;

#define KVSC "sc1"
#define WOAUX 16
#define XBYTES (SEQ * XROW)
#define WBYTES (DM * WROW)
#define SETPRIO(p) __builtin_amdgcn_s_setprio(p)
#define MFMA(a, b, c) __builtin_amdgcn_mfma_f32_32x32x16_bf16((a), (b), (c), 0, 0, 0)
#define SBAR() __builtin_amdgcn_sched_barrier(0)
#define PIN(x) asm volatile("" : "+v"(x))
#define WAIT_BAR(N) asm volatile("s_waitcnt vmcnt(" #N ") lgkmcnt(0)\n\ts_barrier" ::: "memory")
#define RLX_AGENT __ATOMIC_RELAXED, __HIP_MEMORY_SCOPE_AGENT

constexpr int SEQ = 2048, DM = 448, XROW = DM * 2, WROW = 3 * DM * 2;
constexpr unsigned WS_FLAG = 4096, WS_FLAG2 = WS_FLAG + 65536, WS_GRP = 2u << 20, GRP_WOT = 512u << 10, GRP_PAD = 768u << 10, WS_OP = WS_GRP + (16u << 20), WS_END = WS_OP + (4u << 20);
constexpr unsigned WS_DBG = WS_END;
constexpr int SLOT = 32768, LDS_MISC = 4 * SLOT, LDS_GW = LDS_MISC + 8192, LDS_JUNK = LDS_MISC + 16384, LDS_BYTES = LDS_JUNK + 256;
#define FLAGAT 6
constexpr unsigned SPIN_CAP = 1u << 18;
constexpr float QSCALE = 0.18898223650461362f * 1.4426950408889634f;

DI unsigned cvtpk(float lo, float hi) { f32x2 v = {lo, hi}; return __builtin_bit_cast(unsigned, __builtin_convertvector(v, bf16x2)); }
DI bf16x8 pack8(const f32x16& x, int s) {
  u32x4 p = {cvtpk(x[8 * s], x[8 * s + 1]), cvtpk(x[8 * s + 2], x[8 * s + 3]), cvtpk(x[8 * s + 4], x[8 * s + 5]), cvtpk(x[8 * s + 6], x[8 * s + 7])};
  return __builtin_bit_cast(bf16x8, p);
}
DI void dma16(unsigned lds_dst, unsigned voff, rsrc_t rs, unsigned soff) {
  asm volatile("s_mov_b32 m0, %0\n\ts_nop 0\n\tbuffer_load_dwordx4 %1, %2, %3 offen lds" ::"s"(lds_dst), "v"(voff), "s"(rs), "s"(soff) : "memory");
}
DI void dma16_dev(unsigned lds_dst, unsigned voff, rsrc_t rs, unsigned soff) {
  asm volatile("s_mov_b32 m0, %0\n\ts_nop 0\n\tbuffer_load_dwordx4 %1, %2, %3 offen " KVSC " lds" ::"s"(lds_dst), "v"(voff), "s"(rs), "s"(soff) : "memory");
}
DI void await_flags(unsigned long long& f, gu64* p, unsigned long long epoch, unsigned long long need) {
  unsigned n;
  asm volatile("s_mov_b32 %1, 0\n"
               "1:\n\tv_cmp_ne_u64 vcc, %0, %3\n\ts_and_b64 vcc, vcc, %4\n\ts_cbranch_scc0 2f\n\t"
               "s_sleep 1\n\tglobal_load_dwordx2 %0, %2, off sc1\n\ts_add_u32 %1, %1, 1\n\ts_cmp_lt_u32 %1, %5\n\ts_waitcnt vmcnt(0)\n\ts_cbranch_scc1 1b\n"
               "2:" : "+v"(f), "=&s"(n) : "v"(p), "v"(epoch), "s"(need), "s"(SPIN_CAP) : "vcc", "scc", "memory");
}
DI bf16x8 ldsr(lptr p) { return *(const LAS bf16x8*)p; }
DI s16x4 trr(lptr p) { return __builtin_amdgcn_ds_read_tr16_b64_v4i16((LAS s16x4*)p); }
DI bf16x8 trr2(lptr p, int second) { s16x4 lo = trr(p), hi = trr(p + second); return __builtin_shufflevector(lo, hi, 0, 1, 2, 3, 4, 5, 6, 7); }
DI float swap_max(float m) { auto r = __builtin_amdgcn_permlane32_swap(__float_as_uint(m), __float_as_uint(m), false, false); return fmaxf(__uint_as_float(r[0]), __uint_as_float(r[1])); }
DI float rowmax16(const f32x16& s) {
  float a = fmaxf(fmaxf(s[0], s[1]), s[2]), b = fmaxf(fmaxf(s[3], s[4]), s[5]);
  a = fmaxf(fmaxf(a, s[6]), s[7]); b = fmaxf(fmaxf(b, s[8]), s[9]); a = fmaxf(fmaxf(a, s[10]), s[11]); b = fmaxf(fmaxf(b, s[12]), s[13]);
  a = fmaxf(fmaxf(a, s[14]), s[15]);
  return swap_max(fmaxf(a, b));
}
DI float dot2(unsigned a, unsigned b, float c) { return __builtin_amdgcn_fdot2_f32_bf16(__builtin_bit_cast(bf16x2, a), __builtin_bit_cast(bf16x2, b), c, false); }
DI float bflo(unsigned u) { return __uint_as_float(u << 16); }
DI float bfhi(unsigned u) { return __uint_as_float(u & 0xffff0000u); }

#define STAMP(i) do { if ((i) == 0) { SBAR(); xa = __builtin_amdgcn_s_memrealtime(); SBAR(); } if ((i) == 0) { SBAR(); xb = __builtin_amdgcn_s_memrealtime(); SBAR(); } } while (0)
#define ST1(i) do { } while (0)
#define ST2(i) do { } while (0)

__global__ __launch_bounds__(512, 2) void gated_mha_fused(const char* __restrict__ x, const char* __restrict__ wqkvA, const char* __restrict__ woA,
                                                          const char* __restrict__ wqkvB, const char* __restrict__ woB, const char* __restrict__ gate,
                                                          char* __restrict__ out, char* ws) {
  __shared__ __attribute__((aligned(1024))) char lds_raw[LDS_BYTES];
  const unsigned long long hold0 = __builtin_amdgcn_s_memrealtime(); unsigned long long xa = 0, xb = 0;
  STAMP(0);
  const lptr lds = (lptr)lds_raw;
  const unsigned lds0 = (unsigned)(uintptr_t)lds;
  const int tid = threadIdx.x, lane = tid & 63, r32 = lane & 31, hh = lane >> 5;
  const int w = __builtin_amdgcn_readfirstlane(tid >> 6), tt = w & 3, hs = w >> 2;
  const int bid = blockIdx.x, grp = (bid & 7) * 2 + ((bid >> 3) & 1), j = bid >> 4, br = grp >> 3, hp = grp & 7;

  const int gp = bid & 7, pi = (grp & 1) * 16 + j;
  const int ot = 8 * gp + (pi >> 2), cq = pi & 3, gpw = 2 * cq + (gp & 1);
  const unsigned gbase = WS_GRP + (grp << 20), wbase = WS_GRP + (gpw << 21) + GRP_WOT + w * 32768;
  const rsrc_t rsX = __builtin_amdgcn_make_buffer_rsrc((void*)x, 0, XBYTES, 0x00020000);
  const rsrc_t rsW = __builtin_amdgcn_make_buffer_rsrc((void*)(br ? wqkvB : wqkvA), 0, WBYTES, 0x00020000);
  const rsrc_t rsWo = __builtin_amdgcn_make_buffer_rsrc((void*)(hs ? woB : woA), 0, DM * XROW, 0x00020000);
  const rsrc_t rsG = __builtin_amdgcn_make_buffer_rsrc((void*)gate, 0, XROW, 0x00020000);
  const rsrc_t rsWS = __builtin_amdgcn_make_buffer_rsrc((void*)ws, 0, WS_END, 0x00020000);

  const unsigned xrow = 16 * w + (lane >> 3);
  const unsigned xoff0 = (128 * j + xrow) * XROW + 16 * ((lane & 7) ^ ((lane >> 4) & 7));
  const unsigned xoff1 = (128 * j + xrow + 8) * XROW + 16 * ((lane & 7) ^ ((4 + (lane >> 4)) & 7));
  const unsigned wl = (lane >> 3) * WROW + (((lane >> 2) ^ (lane >> 4)) & 1) * 56 + 16 * (lane & 3) + hp * 112;
  const unsigned wkv_s = 16 * tt * WROW + (hs + 1) * XROW;
#define DMA_KV(c, i) do { const unsigned sl_ = lds0 + ((c) & 3) * SLOT + 2048 * w + ((i) >> 1) * 1024; \
    if ((i) & 1) dma16(sl_ + 16384, wl, rsW, wkv_s + (64 * (c) + 8 * ((i) >> 1)) * WROW); else dma16(sl_, ((i) >> 1) ? xoff1 : xoff0, rsX, 128 * (c)); } while (0)
#define DMA_KV4(c) do { DMA_KV(c, 0); DMA_KV(c, 1); DMA_KV(c, 2); DMA_KV(c, 3); } while (0)
#define DMA_QA(q) dma16(lds0 + 3 * SLOT + 4096 * w + 1024 * (q), wl, rsW, (32 * w + 8 * (q)) * WROW)
#define DMA_QB(q) dma16(lds0 + 3072 * w + 1024 * (q), wl, rsW, (256 + 24 * w + 8 * (q)) * WROW)
  const unsigned op_own = WS_OP + ((j * 4 + tt) * 32 + 2 * grp + hs) * 2048;
  DMA_KV4(0);
  unsigned gw;
  asm volatile("buffer_load_ushort %0, %1, %2, 0 offen" : "=v"(gw) : "v"(tid * 2), "s"(rsG) : "memory");
  {
    const char* tp = w == 0 ? x + (size_t)min(bid * 56 + lane, 14335) * 128
                   : w < 3 ? (w == 1 ? wqkvA : wqkvB) + (size_t)min(bid * 37 + min(lane, 36), 9407) * 128
                   : w < 5 ? (w == 3 ? woA : woB) + (size_t)min(bid * 13 + min(lane, 12), 3135) * 128
                   : w < 7 ? ws + (w == 5 ? WS_GRP + ((hh ? gpw : gp) << 21) + GRP_PAD : hh ? op_own : WS_OP + ot * 65536) : out + (size_t)(32 * ot + 31 * hh) * XROW;
    asm volatile("s_mov_b32 m0, %0\n\ts_nop 0\n\tglobal_load_lds_dword %1, off sc1" ::"s"(lds0 + LDS_JUNK), "v"(tp) : "memory"); }
  DMA_KV4(1);
  unsigned long long epoch = __hip_atomic_load((gu64*)ws, RLX_AGENT);
  asm volatile("" ::: "memory");
#define DMA_WO() dma16(lds0 + LDS_MISC + 1024 * w, (lane >> 2) * XROW + 16 * (lane & 3), rsWo, (28 * (4 * tt + (pi >> 3)) + 16 * ((pi >> 2) & 1)) * XROW + 224 * (gp >> 1) + 64 * (pi & 3))

  bf16x8 xf[28], wk[2], wv[2];
  f32x16 accK = {}, accV = {};
  const unsigned xr = (32 * tt + r32) * 128 + ((hh ^ ((r32 >> 1) & 7)) * 16);
  const unsigned trl = (8 * hh + ((lane & 15) >> 2)) * 128 + ((hs ^ (lane >> 3)) & 1) * 64 + ((lane >> 4) & 1) * 32 + (lane & 3) * 8;
  lptr ldh = lds + 2 * SLOT; PIN(ldh);
#define KSL(c) ((((c) & 2) ? ldh : lds) + ((c) & 1) * SLOT)
  const bool gl = (lane & 19) == 19;
  const unsigned kdl = gl ? 8u : (unsigned)SLOT, kwrap = gl ? 8u : (unsigned)(-3 * SLOT);
  lptr ak = lds + (gl ? LDS_GW + (trl & ~127u) : 16384 + trl);
#define KV_RD(n) do { const lptr s_ = KSL((n) >> 2); xf[n] = ldsr(s_ + (xr ^ (32 * ((n) & 3)))); \
    if ((n) && !((n) & 3)) { ak += (n) == 16 ? kwrap : kdl; PIN(ak); } \
    wk[(n) & 1] = trr2(ak + ((n) & 3) * 2048, 512); wv[(n) & 1] = trr2(s_ + 24576 + trl + ((n) & 3) * 2048, 512); } while (0)
#define KV_STEP(n, MEM) do { SBAR(); if ((n) < 27) KV_RD((n) + 1); SBAR(); \
    accK = MFMA(wk[(n) & 1], xf[n], accK); accV = MFMA(xf[n], wv[(n) & 1], accV); SBAR(); MEM; SBAR(); } while (0)
  asm volatile("s_waitcnt vmcnt(6)" : "+v"(gw) :: "memory");
  *(LAS unsigned short*)(lds + LDS_GW + lane * 128 + w * 8) = (unsigned short)gw;
  WAIT_BAR(6); STAMP(1); ST1(1); KV_RD(0);
  KV_STEP(0, DMA_KV(2, 0); DMA_KV(2, 1)); KV_STEP(1, DMA_KV(2, 2)); KV_STEP(2, DMA_KV(2, 3)); WAIT_BAR(5); ST1(2);
  KV_STEP(3, DMA_KV(3, 0)); KV_STEP(4, DMA_KV(3, 1)); KV_STEP(5, DMA_KV(3, 2)); KV_STEP(6, DMA_KV(3, 3)); WAIT_BAR(4); ST1(3);
  KV_STEP(7, DMA_KV(4, 0)); KV_STEP(8, DMA_KV(4, 1)); KV_STEP(9, DMA_KV(4, 2)); KV_STEP(10, DMA_KV(4, 3)); WAIT_BAR(4); ST1(4);
  KV_STEP(11, DMA_KV(5, 0)); KV_STEP(12, DMA_KV(5, 1)); KV_STEP(13, DMA_KV(5, 2)); KV_STEP(14, DMA_KV(5, 3)); WAIT_BAR(4); ST1(5);
  KV_STEP(15, DMA_KV(6, 0)); KV_STEP(16, DMA_KV(6, 1)); KV_STEP(17, DMA_KV(6, 2)); KV_STEP(18, DMA_KV(6, 3)); WAIT_BAR(4); ST1(6);
  KV_STEP(19, DMA_QA(0)); KV_STEP(20, (void)0); KV_STEP(21, DMA_QA(1)); KV_STEP(22, (void)0); WAIT_BAR(2); ST1(7);
  KV_STEP(23, DMA_QA(2)); KV_STEP(24, (void)0); KV_STEP(25, DMA_QA(3)); KV_STEP(26, (void)0); KV_STEP(27, (void)0);
  STAMP(2); ST1(8);
  bf16x8 k0 = pack8(accK, 0), k1 = pack8(accK, 1), v0 = pack8(accV, 0), v1 = pack8(accV, 1);
  { u32x4 t = __builtin_bit_cast(u32x4, k1); t[2] = hh ? 0x00003f80u : t[2]; t[3] = hh ? 0u : t[3]; k1 = __builtin_bit_cast(bf16x8, t);
    u32x4 a = __builtin_bit_cast(u32x4, v0), b = __builtin_bit_cast(u32x4, v1); const unsigned pv = r32 == 28 ? 0x3f803f80u : 0u;
#pragma unroll
    for (int i = 0; i < 4; ++i) { a[i] = r32 >= 28 ? pv : a[i]; b[i] = r32 >= 28 ? pv : b[i]; }
    v0 = __builtin_bit_cast(bf16x8, a); v1 = __builtin_bit_cast(bf16x8, b); }
  { const unsigned kvo = gbase + j * SLOT + w * 4096;
    __builtin_amdgcn_raw_buffer_store_b128(__builtin_bit_cast(u32x4, k0), rsWS, lane * 16, kvo, 16);
    __builtin_amdgcn_raw_buffer_store_b128(__builtin_bit_cast(u32x4, k1), rsWS, lane * 16 + 1024, kvo, 16);
    __builtin_amdgcn_raw_buffer_store_b128(__builtin_bit_cast(u32x4, v0), rsWS, lane * 16 + 2048, kvo, 16);
    __builtin_amdgcn_raw_buffer_store_b128(__builtin_bit_cast(u32x4, v1), rsWS, lane * 16 + 3072, kvo, 16);
    const lptr own = lds + SLOT + w * 4096 + lane * 16;
    *(LAS bf16x8*)own = k0; *(LAS bf16x8*)(own + 1024) = k1; *(LAS bf16x8*)(own + 2048) = v0; *(LAS bf16x8*)(own + 3072) = v1; }
  float gs;
  { auto r = __builtin_amdgcn_permlane32_swap(__float_as_uint(accK[12]), __float_as_uint(accK[12]), false, false);
    gs = 1.0f / (1.0f + __expf(-__uint_as_float(r[1]))); gs = br ? 1.0f - gs : gs; }
  f32x16 accQ = {};
  WAIT_BAR(4); ST1(9);
#pragma unroll
  for (int ks = 0; ks < FLAGAT; ++ks) accQ = MFMA(trr2(ldh + SLOT + trl + ks * 2048, 512), xf[ks], accQ);
  PIN(accQ);
  asm volatile("s_waitcnt vmcnt(0)" ::: "memory");
  PIN(epoch); epoch += 1;
  if (lane == 0) __hip_atomic_store((gu64*)(ws + WS_FLAG + (gp * 8 + w) * 1024 + pi * 32), epoch, RLX_AGENT);
  DMA_QB(0); DMA_QB(1); DMA_QB(2); DMA_WO();
  ST1(10);
#pragma unroll
  for (int ks = FLAGAT; ks < 16; ++ks) accQ = MFMA(trr2(ldh + SLOT + trl + ks * 2048, 512), xf[ks], accQ);
  PIN(accQ);
  gu64* fp = (gu64*)(ws + WS_FLAG + (gp * 8 + w) * 1024 + ((grp & 1) * 16 + (lane & 15)) * 32);
  STAMP(3);
  WAIT_BAR(1);
  unsigned long long kflag;
#define POLL() asm volatile("global_load_dwordx2 %0, %1, off sc1" : "=v"(kflag) : "v"(fp) : "memory")
  POLL();
#pragma unroll
  for (int ks = 0; ks < 12; ++ks) accQ = MFMA(trr2(lds + trl + ks * 2048, 512), xf[16 + ks], accQ);
#pragma unroll
  for (int i = 0; i < 16; ++i) accQ[i] *= QSCALE;
  bf16x8 q0 = pack8(accQ, 0), q1 = pack8(accQ, 1);
  { u32x4 t = __builtin_bit_cast(u32x4, q1); t[2] = hh ? 0u : t[2]; t[3] = hh ? 0u : t[3]; q1 = __builtin_bit_cast(bf16x8, t); }
  const bf16x8 q1z = q1;
  const f32x16 zero = {};
  {
    f32x16 s = MFMA(k0, q0, zero); s = MFMA(k1, q1, s);
    const float m = rowmax16(s);
    u32x4 t = __builtin_bit_cast(u32x4, q1); t[2] = hh ? (cvtpk(-m, 0.f) & 0xffffu) : t[2]; q1 = __builtin_bit_cast(bf16x8, t); }
  PIN(q1);
  STAMP(4);

  asm volatile("s_waitcnt vmcnt(0)" ::: "memory");
  await_flags(kflag, fp, epoch, (j < 3 ? 15ull : 7ull) & ~(1ull << j));
  STAMP(5); ST2(1);
  bf16x8 wot;
  { const lptr wp = lds + LDS_MISC + w * 1024 + (4 * hh + ((lane & 15) >> 2)) * 64 + ((lane >> 4) & 1) * 32 + (lane & 3) * 8; wot = trr2(wp, 512); }
  const unsigned kvl = lane * 16, kvd = lds0 + w * 4096, kvs = gbase + w * 4096;
#define DMA_P(s, p) dma16_dev(kvd + (((s) + 1) & 3) * SLOT + (p) * 1024, kvl, rsWS, kvs + ((s) - 1 + ((s) - 1 >= j)) * SLOT + (p) * 1024)
  DMA_P(1, 0); DMA_P(1, 1); DMA_P(1, 2); DMA_P(1, 3); DMA_P(2, 0); DMA_P(2, 1); DMA_P(2, 2); DMA_P(2, 3);
  bf16x8 wo[32];
#define WO_LD(ks) do { wo[ks] = __builtin_bit_cast(bf16x8, __builtin_amdgcn_raw_buffer_load_b128(rsWS, kvl, wbase + (ks) * 1024, WOAUX)); asm volatile("" ::: "memory"); } while (0)

  f32x16 o = {}, sa, sb;
  u32x4 p0, p1 = {0u, 0u, 0u, 0u};
  const lptr fb = lds + hs * 16384 + lane * 16;
  lptr fbh = fb + 2 * SLOT; PIN(fbh);
#define SL(s) ((((s) + 1) & 2) ? fbh + (((s) + 1) & 1) * SLOT : fb + (((s) + 1) & 1) * SLOT)
  k0 = ldsr(SL(0)); k1 = ldsr(SL(0) + 1024);
  sa = MFMA(k0, q0, zero); sa = MFMA(k1, q1, sa);
  k0 = ldsr(SL(0) + 4096); k1 = ldsr(SL(0) + 4096 + 1024); v0 = ldsr(SL(0) + 2048); v1 = v0;
#define EX(S, i) do { S[i] = __builtin_amdgcn_exp2f(S[i]); } while (0)
#define STEP(S, T, KP, V1P, V0P, PRIO, MEM0, MEM1) do { \
    SBAR(); SETPRIO(PRIO); EX(S, 0); EX(S, 1); PIN(S); SBAR(); \
    T = MFMA(k0, q0, zero); k0 = ldsr(KP); SBAR(); \
    EX(S, 2); p0[0] = cvtpk(S[0], S[1]); EX(S, 3); EX(S, 4); p0[1] = cvtpk(S[2], S[3]); EX(S, 5); PIN(S); PIN(p0); SBAR(); \
    T = MFMA(k1, q1, T); k1 = ldsr((KP) + 1024); SBAR(); MEM0; SBAR(); \
    EX(S, 6); p0[2] = cvtpk(S[4], S[5]); EX(S, 7); EX(S, 8); p0[3] = cvtpk(S[6], S[7]); EX(S, 9); PIN(S); PIN(p0); SBAR(); \
    o = MFMA(v1, __builtin_bit_cast(bf16x8, p1), o); v1 = ldsr(V1P); SBAR(); \
    EX(S, 10); p1[0] = cvtpk(S[8], S[9]); EX(S, 11); EX(S, 12); p1[1] = cvtpk(S[10], S[11]); EX(S, 13); PIN(S); PIN(p1); SBAR(); \
    o = MFMA(v0, __builtin_bit_cast(bf16x8, p0), o); v0 = ldsr(V0P); SBAR(); MEM1; SBAR(); \
    EX(S, 14); p1[2] = cvtpk(S[12], S[13]); EX(S, 15); p1[3] = cvtpk(S[14], S[15]); PIN(p1); SBAR(); } while (0)
  const u32x4 sent = {~0u, ~0u, ~0u, ~0u};
#define SENT(i) do { __builtin_amdgcn_raw_buffer_store_b128(sent, rsWS, kvl + 1024 * (i), op_own, 16); asm volatile("" ::: "memory"); } while (0)
#define WOT_ST() do { __builtin_amdgcn_raw_buffer_store_b128(__builtin_bit_cast(u32x4, wot), rsWS, kvl, WS_GRP + (gp << 21) + GRP_WOT + w * 32768 + pi * 1024, 16); asm volatile("" ::: "memory"); } while (0)
#define STAGE(s) do { \
    STEP(sa, sb, SL(s) + 2 * 4096, SL(s) + 3072, SL(s) + 4096 + 2048, 1, if ((s) == 0) SENT(0); else if ((s) <= 13) DMA_P((s) + 2, 2), \
         if ((s) >= 5 && (s) <= 14) WO_LD(3 * (s) - 13); else if ((s) == 0) WOT_ST()); \
    STEP(sb, sa, SL(s) + 3 * 4096, SL(s) + 4096 + 3072, SL(s) + 2 * 4096 + 2048, 0, if ((s) == 0) SENT(1); else if ((s) <= 13) DMA_P((s) + 2, 3), \
         if ((s) >= 5 && (s) <= 14) WO_LD(3 * (s) - 12); else if ((s) == 0 || (s) == 3) POLL()); \
    asm volatile("s_waitcnt vmcnt(%0)\n\ts_barrier" ::"n"((s) == 0 ? 8 : (s) == 1 ? 4 : (s) <= 3 ? 5 : (s) == 4 ? 4 : (s) <= 13 ? 8 : (s) == 14 ? 4 : 63) : "memory"); \
    if ((s) < 3) ST2(2 + (s)); \
    if ((s) == 1) { await_flags(kflag, fp, epoch, 0xffffull); \
        \
      if (lane == 0) __hip_atomic_store((gu64*)(ws + WS_FLAG2 + (gp * 8 + w) * 1024 + pi * 32), epoch, RLX_AGENT); \
      fp = (gu64*)(ws + WS_FLAG2 + (hh ? (gpw * 8 + w) * 1024 + r32 * 32 : (w * 8 + (lane & 1) * 4 + (ot & 3)) * 1024 + ((lane & 2) * 8 + (ot >> 2)) * 32)); } \
    if ((s) == 4) await_flags(kflag, fp, epoch, ~0ull); \
    STEP(sa, sb, SL((s) + 1), SL(s) + 2 * 4096 + 3072, SL(s) + 3 * 4096 + 2048, 3, if ((s) <= 12) DMA_P((s) + 3, 0), if ((s) >= 4 && (s) <= 14) WO_LD((s) == 4 ? 0 : 3 * (s) - 11)); \
    STEP(sb, sa, SL((s) + 1) + 4096, SL(s) + 3 * 4096 + 3072, SL((s) + 1) + 2048, 2, if ((s) <= 12) DMA_P((s) + 3, 1), if ((s) == 4) WO_LD(1)); } while (0)
  STAGE(0); STAGE(1); STAGE(2); STAGE(3); STAGE(4); STAGE(5); STAGE(6); STAGE(7);
  STAGE(8); STAGE(9); STAGE(10); STAGE(11); STAGE(12); STAGE(13); STAGE(14); STAGE(15);
  SETPRIO(0);
  o = MFMA(v1, __builtin_bit_cast(bf16x8, p1), o); PIN(o);
  STAMP(6); ST2(5);

  float l;
  { auto r = __builtin_amdgcn_permlane32_swap(__float_as_uint(o[12]), __float_as_uint(o[12]), false, false); l = __uint_as_float(r[1]); }
  { float amax = l;
#pragma unroll
    for (int i = 0; i < 16; ++i) amax = fmaxf(amax, fabsf(o[i]));
    bool bad = !(amax < 1e30f) || !(l > 1e-30f);
#pragma unroll
    for (int i = 0; i < 16; ++i) bad |= (o[i] != o[i]);
    if (__builtin_expect(__any(bad), 0)) {
      float m = -INFINITY; o = zero;
      for (int t = 0; t < 64; ++t) {
        const unsigned ko = gbase + (t >> 2) * SLOT + (hs * 4 + (t & 3)) * 4096;
        const bf16x8 a0 = __builtin_bit_cast(bf16x8, __builtin_amdgcn_raw_buffer_load_b128(rsWS, kvl, ko, 16));
        const bf16x8 a1 = __builtin_bit_cast(bf16x8, __builtin_amdgcn_raw_buffer_load_b128(rsWS, kvl + 1024, ko, 16));
        const bf16x8 b0 = __builtin_bit_cast(bf16x8, __builtin_amdgcn_raw_buffer_load_b128(rsWS, kvl + 2048, ko, 16));
        const bf16x8 b1 = __builtin_bit_cast(bf16x8, __builtin_amdgcn_raw_buffer_load_b128(rsWS, kvl + 3072, ko, 16));
        f32x16 s = MFMA(a0, q0, zero); s = MFMA(a1, q1z, s);
        const float mn = fmaxf(m, rowmax16(s)), al = __builtin_amdgcn_exp2f(m - mn); m = mn;
#pragma unroll
        for (int i = 0; i < 16; ++i) { o[i] *= al; s[i] = __builtin_amdgcn_exp2f(s[i] - mn); }
        o = MFMA(b0, pack8(s, 0), o); o = MFMA(b1, pack8(s, 1), o);
      }
      auto r = __builtin_amdgcn_permlane32_swap(__float_as_uint(o[12]), __float_as_uint(o[12]), false, false); l = __uint_as_float(r[1]);
    } }
  { const float inv = gs / l;
#pragma unroll
    for (int i = 0; i < 16; ++i) o[i] *= inv;
    u32x4 a = __builtin_bit_cast(u32x4, pack8(o, 0)), b = __builtin_bit_cast(u32x4, pack8(o, 1));
    b[2] = hh ? 0u : b[2]; b[3] = hh ? 0u : b[3];
#pragma unroll
    for (int i = 0; i < 4; ++i) { a[i] = min(a[i], 0xfffffffeu); b[i] = min(b[i], 0xfffffffeu); }
    __builtin_amdgcn_raw_buffer_store_b128(a, rsWS, lane * 16, op_own, 16); __builtin_amdgcn_raw_buffer_store_b128(b, rsWS, lane * 16 + 1024, op_own, 16); }
  __builtin_amdgcn_s_barrier();
  STAMP(7); ST2(6);

  f32x16 y[4] = {};
  const lptr rb = lds + SLOT + lane * 16;
  asm volatile("" ::: "memory"); ST2(7);
  { const unsigned po = WS_OP + ot * 65536 + w * 8192; u32x4 pl[8];
#pragma unroll
    for (int i = 0; i < 8; ++i) pl[i] = __builtin_amdgcn_raw_buffer_load_b128(rsWS, kvl, po + 1024 * i, 16);
#pragma unroll
    for (int hf = 0; hf < 2; ++hf) {
      for (unsigned spins = 0; spins < SPIN_CAP; ++spins) {
        unsigned mx = 0;
#pragma unroll
        for (int i = 4 * hf; i < 4 * hf + 4; ++i) mx = max(max(mx, max(pl[i][0], pl[i][1])), max(pl[i][2], pl[i][3]));
        if (!__any(mx == ~0u)) break;
        __builtin_amdgcn_s_sleep(4);
        asm volatile("" ::: "memory");
#pragma unroll
        for (int i = 4 * hf; i < 4 * hf + 4; ++i) pl[i] = __builtin_amdgcn_raw_buffer_load_b128(rsWS, kvl, po + 1024 * i, 16);
      }
      if (hf == 0) {
        ST2(8);
#pragma unroll
        for (int i = 0; i < 4; ++i)
#pragma unroll
          for (int nt = 0; nt < 4; ++nt) y[nt] = MFMA(wo[4 * i + nt], __builtin_bit_cast(bf16x8, pl[i]), y[nt]);
        PIN(y[0]); PIN(y[1]); PIN(y[2]); PIN(y[3]);
      } else {
#pragma unroll
        for (int nt = 0; nt < 5; ++nt) {
          SBAR();
          if (nt < 4)
#pragma unroll
            for (int i = 4; i < 8; ++i) y[nt] = MFMA(wo[4 * i + nt], __builtin_bit_cast(bf16x8, pl[i]), y[nt]);
          SBAR();
          if (nt > 0)
#pragma unroll
            for (int u = 4 * nt - 4; u < min(4 * nt, 14); ++u)
              *(LAS f32x4*)(rb + w * 14336 + u * 1024) = (f32x4){y[u >> 2][4 * (u & 3)], y[u >> 2][4 * (u & 3) + 1], y[u >> 2][4 * (u & 3) + 2], y[u >> 2][4 * (u & 3) + 3]};
        }
      }
    } }
  STAMP(8); ST2(9);
  {
    asm volatile("s_waitcnt lgkmcnt(0)\n\ts_barrier" ::: "memory");
    STAMP(9);
    if (bid == 0 && tid == 0) __hip_atomic_store((gu64*)ws, epoch, RLX_AGENT);
    if (w != 7) {
      unsigned r[4];
#pragma unroll
      for (int u = 0; u < 2; ++u) { f32x4 t[8];
#pragma unroll
        for (int k = 0; k < 8; ++k) t[k] = *(const LAS f32x4*)(rb + k * 14336 + (2 * w + u) * 1024);
        const f32x4 a = (t[0] + t[1]) + (t[2] + t[3]), b = (t[4] + t[5]) + (t[6] + t[7]), m = a + b;
        r[2 * u] = cvtpk(m[0], m[1]); r[2 * u + 1] = cvtpk(m[2], m[3]); }
      auto e0 = __builtin_amdgcn_permlane32_swap(r[0], r[2], false, false), e1 = __builtin_amdgcn_permlane32_swap(r[1], r[3], false, false);
      *(u32x4*)(out + (size_t)(32 * ot + r32) * XROW + (112 * cq + 16 * w + 8 * hh) * 2) = (u32x4){e0[0], e1[0], e0[1], e1[1]};
    } }
  STAMP(10);
  if (bid == 0 && w == 0) { const unsigned long long e_ = hold0 + 6000 + (xb - xa) / 1; for (int n_ = 0; n_ < 400000 && __builtin_amdgcn_s_memrealtime() < e_; ++n_) __builtin_amdgcn_s_sleep(1); }
}

extern "C" void kernel_launch(void* const* d_in, const int* in_sizes, int n_in, void* d_out, int out_size, void* d_ws, size_t ws_size,
                              hipStream_t stream) {
  gated_mha_fused<<<256, 512, 0, stream>>>((const char*)d_in[0], (const char*)d_in[1], (const char*)d_in[2], (const char*)d_in[4],
                                           (const char*)d_in[5], (const char*)d_in[6], (char*)d_out, (char*)d_ws);
}
```

```cpp
#include <hip/hip_runtime.h>
#include <hip/hip_bf16.h>
#include <stdint.h>

typedef short    bf16x8 __attribute__((ext_vector_type(8)));
typedef short    s16x4  __attribute__((ext_vector_type(4)));
typedef float    f32x16 __attribute__((ext_vector_type(16)));
typedef float    f32x2  __attribute__((ext_vector_type(2)));
typedef float    f32x4  __attribute__((ext_vector_type(4)));
typedef __bf16   bf16x2 __attribute__((ext_vector_type(2)));
typedef unsigned u32x2  __attribute__((ext_vector_type(2)));
typedef unsigned u32x4  __attribute__((ext_vector_type(4)));
typedef unsigned long long u64x8 __attribute__((ext_vector_type(8)));
typedef __bf16   bf8    __attribute__((ext_vector_type(8)));
typedef __bf16   bf16v  __attribute__((ext_vector_type(16)));
typedef __amdgpu_buffer_rsrc_t rsrc_t;
#define DI __device__ __forceinline__
#define LAS __attribute__((address_space(3)))
#define GAS __attribute__((address_space(1)))
typedef LAS char* lptr;
typedef GAS unsigned long long gu64;

#define SETPRIO(p) __builtin_amdgcn_s_setprio(p)
#define MFMA(a, b, c) __builtin_amdgcn_mfma_f32_32x32x16_bf16((a), (b), (c), 0, 0, 0)
#define SBAR() __builtin_amdgcn_sched_barrier(0)
#define PIN(x) asm volatile("" : "+v"(x))
#define WAIT_BAR(N) asm volatile("s_waitcnt vmcnt(" #N ") lgkmcnt(0)\n\ts_barrier" ::: "memory")
#define RLX_AGENT __ATOMIC_RELAXED, __HIP_MEMORY_SCOPE_AGENT

constexpr int SEQ = 2048, DM = 448, XROW = DM * 2, WROW = 3 * DM * 2;
constexpr unsigned WS_FLAG = 4096, WS_FLAG2 = WS_FLAG + 65536, WS_GRP = 2u << 20, GRP_WOT = 1u << 20, GRP_PAD = 3u << 19, WS_OP = WS_GRP + (16u << 20), WS_END = WS_OP + (4u << 20);
constexpr int SLOT = 32768, LDS_WV = 3 * SLOT, LDS_JUNK = LDS_WV + 448 * 64, LDS_W = 4 * SLOT, LDS_V4 = LDS_W + 8192, LDS_GW = LDS_W + 3 * 8192, LDS_BYTES = LDS_GW + 4096;
constexpr unsigned SPIN_CAP = 1u << 18;
constexpr int DEV = 16;
constexpr float LOG2E = 1.4426950408889634f, QSCALE = 0.18898223650461362f * LOG2E;

DI unsigned cvtpk(float lo, float hi) { f32x2 v = {lo, hi}; return __builtin_bit_cast(unsigned, __builtin_convertvector(v, bf16x2)); }
DI bf16x8 pack8(const f32x16& x, int s) {
  u32x4 p = {cvtpk(x[8 * s], x[8 * s + 1]), cvtpk(x[8 * s + 2], x[8 * s + 3]), cvtpk(x[8 * s + 4], x[8 * s + 5]), cvtpk(x[8 * s + 6], x[8 * s + 7])};
  return __builtin_bit_cast(bf16x8, p);
}
DI void dma16(unsigned lds_dst, unsigned voff, rsrc_t rs, unsigned soff) {
  asm volatile("s_mov_b32 m0, %0\n\ts_nop 0\n\tbuffer_load_dwordx4 %1, %2, %3 offen lds" ::"s"(lds_dst), "v"(voff), "s"(rs), "s"(soff) : "memory");
}
template <int OFF> DI void dma16_dev(unsigned lds_dst, unsigned voff, rsrc_t rs, unsigned soff) {
  asm volatile("s_mov_b32 m0, %0\n\ts_nop 0\n\tbuffer_load_dwordx4 %1, %2, %3 offen offset:%4 sc1 lds" ::"s"(lds_dst), "v"(voff), "s"(rs), "s"(soff), "n"(OFF) : "memory");
}
DI void await_flags(unsigned long long& f, unsigned fo, rsrc_t rs, unsigned long long epoch, unsigned long long need) {
  unsigned n;
  asm volatile("s_mov_b32 %1, 0\n"
               "1:\n\tv_cmp_ne_u64 vcc, %0, %3\n\ts_and_b64 vcc, vcc, %4\n\ts_cbranch_scc0 2f\n\t"
               "s_sleep 1\n\tbuffer_load_dwordx2 %0, %2, %6, 0 offen sc1\n\ts_add_u32 %1, %1, 1\n\ts_cmp_lt_u32 %1, %5\n\ts_waitcnt vmcnt(0)\n\ts_cbranch_scc1 1b\n"
               "2:" : "+v"(f), "=&s"(n) : "v"(fo), "v"(epoch), "s"(need), "s"(SPIN_CAP), "s"(rs) : "vcc", "scc", "memory");
}
DI bf16x8 ldsr(lptr p) { return *(const LAS bf16x8*)p; }
DI s16x4 trr(lptr p) { return __builtin_amdgcn_ds_read_tr16_b64_v4i16((LAS s16x4*)p); }
DI bf16x8 trr2(lptr p, int second) { s16x4 lo = trr(p), hi = trr(p + second); return __builtin_shufflevector(lo, hi, 0, 1, 2, 3, 4, 5, 6, 7); }
DI float swap_max(float m) { auto r = __builtin_amdgcn_permlane32_swap(__float_as_uint(m), __float_as_uint(m), false, false); return fmaxf(__uint_as_float(r[0]), __uint_as_float(r[1])); }
DI float rowmax16(const f32x16& s) {
  float a = fmaxf(fmaxf(s[0], s[1]), s[2]), b = fmaxf(fmaxf(s[3], s[4]), s[5]);
  a = fmaxf(fmaxf(a, s[6]), s[7]); b = fmaxf(fmaxf(b, s[8]), s[9]); a = fmaxf(fmaxf(a, s[10]), s[11]); b = fmaxf(fmaxf(b, s[12]), s[13]);
  a = fmaxf(fmaxf(a, s[14]), s[15]);
  return swap_max(fmaxf(a, b));
}

__global__ __launch_bounds__(512, 2) void gated_mha_fused(const char*, const char*, const char*, const char*, const char*, const char*, char*, char*) {
  u64x8 ka;
  asm volatile("s_load_dwordx16 %0, %1, 0" : "=s"(ka) : "s"(__builtin_amdgcn_kernarg_segment_ptr())); SBAR();
  __shared__ __attribute__((aligned(1024))) char lds_raw[LDS_BYTES];
  const lptr lds = (lptr)lds_raw;
  const unsigned lds0 = (unsigned)(uintptr_t)lds;
  const int tid = threadIdx.x, lane = tid & 63, r32 = lane & 31, hh = lane >> 5;
  const int w = __builtin_amdgcn_readfirstlane(tid >> 6);
  const int bid = blockIdx.x, gp = bid & 7, pi = bid >> 3, jb = pi >> 2, hq = 4 * gp + (pi & 3), br = gp >> 2;
#define HEAD(q, sl) (4 * (q) + ((0xd8d88db4u >> (8 * (q) + 2 * (sl))) & 3))
  const int ot = 8 * gp + (pi >> 2), cq = pi & 3, gpw = 2 * cq + (gp & 1);
  const unsigned gbase = WS_GRP + (gp << 21) + ((pi & 3) << 18), wbase = WS_GRP + (gpw << 21) + GRP_WOT + w * 32768;
  const unsigned xrow = 256 * jb + 32 * w + (lane >> 3);
  unsigned xoff0 = xrow * XROW + 16 * ((lane & 7) ^ ((lane >> 4) & 7));
  unsigned xoff1 = (xrow + 8) * XROW + 16 * ((lane & 7) ^ ((4 + (lane >> 4)) & 7));
  unsigned wl = (lane >> 2) * WROW + min(16 * (lane & 3), 40) + HEAD(gp & 3, pi & 3) * 56;
  unsigned wkq_s = 16 * (w & 3) * WROW + (w >> 2 ? 0 : XROW);
#define DMA_X(c, i) dma16(lds0 + ((c) % 3) * SLOT + 4096 * w + 1024 * (i), ((i) & 1) ? xoff1 : xoff0, rsX, 128 * (c) + ((i) >> 1) * 16 * XROW)
#define DMA_W(c) dma16(lds0 + LDS_W + ((c) % 3) * 8192 + 1024 * w, wl, rsW, wkq_s + 64 * (c) * WROW)
#define DMA_CHUNK(c) do { DMA_W(c); DMA_X(c, 0); DMA_X(c, 1); DMA_X(c, 2); DMA_X(c, 3); } while (0)
#define DMA_V(q) dma16(lds0 + LDS_WV + 8192 * (q) + 1024 * w, wl, rsW, (128 * (q) + 16 * ((q) == 3 ? w & 3 : w)) * WROW + 2 * XROW)
  const unsigned op_own = WS_OP + ((8 * jb + w) * 32 + hq) * 2048;
  const int role = w == 7 || (pi & 2) ? 4 : w >> 1;
  unsigned t2 = min(tid, DM - 1) * 2;
  PIN(xoff0); PIN(xoff1); PIN(wl); PIN(t2); asm volatile("" : "+s"(wkq_s)); SBAR();
  asm volatile("s_waitcnt lgkmcnt(0)" : "+s"(ka));
  unsigned long long tp0, tp1;
  asm volatile("s_load_dwordx2 %0, %2, %3\n\ts_load_dwordx2 %1, %2, %4" : "=&s"(tp0), "=&s"(tp1)
               : "s"(__builtin_amdgcn_kernarg_segment_ptr()), "s"(((0x72310u >> (4 * role)) & 7) * 8), "s"(((0x64310u >> (4 * role)) & 7) * 8));
  const char *x = (const char*)ka[0], *wqkvA = (const char*)ka[1], *woA = (const char*)ka[2], *wqkvB = (const char*)ka[3], *woB = (const char*)ka[4], *gate = (const char*)ka[5];
  char *out = (char*)ka[6]; GAS char* ws = (GAS char*)ka[7];
  const rsrc_t rsX = __builtin_amdgcn_make_buffer_rsrc((void*)x, 0, SEQ * XROW, 0x00020000);
  const rsrc_t rsW = __builtin_amdgcn_make_buffer_rsrc((void*)(br ? wqkvB : wqkvA), 0, DM * WROW, 0x00020000);
  const rsrc_t rsWo = __builtin_amdgcn_make_buffer_rsrc((void*)(w >> 2 ? woB : woA), 0, DM * XROW, 0x00020000);
  const rsrc_t rsG = __builtin_amdgcn_make_buffer_rsrc((void*)gate, 0, XROW, 0x00020000);
  const rsrc_t rsWS = __builtin_amdgcn_make_buffer_rsrc((void*)ka[7], 0, WS_END, 0x00020000);
  DMA_CHUNK(0);
  unsigned gw;
  asm volatile("buffer_load_ushort %0, %1, %2, 0 offen" : "=v"(gw) : "v"(t2), "s"(rsG) : "memory");
  {
    const int fi = (2 * ((bid >> 5) * 16 + (bid & 15)) + (w & 1)) >> (role == 3), per = role == 0 ? 56 : role == 3 ? 25 : 37;
    unsigned toff = 128 * min(fi * per + min(lane & (role == 3 ? 31 : 63), per - 1), role == 0 ? 14335 : role == 3 ? 3135 : 9407);
    unsigned a = lane & 1 ? (32 * ot + 31) * XROW : 32 * ot * XROW, b = lane & 1 ? WS_GRP + GRP_PAD + (gpw << 21) : WS_GRP + GRP_PAD + (gp << 21), c = lane & 1 ? op_own : WS_OP + ot * 65536;
    PIN(a); PIN(b); PIN(c); PIN(toff); c = lane & 2 ? b : c; c = lane < 2 ? a : c; toff = role == 4 ? c : toff;
    asm volatile("s_waitcnt lgkmcnt(0)" : "+s"(tp0), "+s"(tp1));
    asm volatile("s_mov_b32 m0, %0\n\ts_nop 0\n\tglobal_load_lds_dword %1, off sc1" ::"s"(lds0 + LDS_JUNK), "v"(((role == 4 ? lane < 2 : hh) ? tp1 : tp0) + toff) : "memory"); }
  DMA_CHUNK(1);
  unsigned long long epoch;
  asm volatile("global_load_dwordx2 %0, %1, %2 sc1" : "=v"(epoch) : "v"(0), "s"(ws) : "memory");
#define DMA_WO() dma16(lds0 + LDS_W + 1024 * w, wov, rsWo, wos)
  unsigned wov = (lane >> 2) * XROW + 16 * (lane & 3), wos = (28 * HEAD(w & 3, pi >> 3) + 16 * ((pi >> 2) & 1)) * XROW + 224 * (gp >> 1) + 64 * (pi & 3);
  unsigned l0 = lane ? 0x80000000u : 0u;
  unsigned f1s = WS_FLAG + (gp * 8 + w) * 1024 + ((pi & 3) * 8 + jb) * 32, f2s = WS_FLAG2 + (gp * 8 + w) * 1024 + pi * 32;
  unsigned fp = WS_FLAG + (gp * 8 + w) * 1024 + ((pi & 3) * 8 + (lane & 7)) * 32;
  unsigned fp2 = WS_FLAG2 + (hh ? (gpw * 8 + w) * 1024 + r32 * 32 : (w * 8 + (ot & 7)) * 1024 + ((ot >> 3) * 4 + (lane & 3)) * 32);
  unsigned po = __builtin_amdgcn_readfirstlane(WS_OP + ot * 65536 + w * 8192), po4 = po + 4096, wots = WS_GRP + (gp << 21) + GRP_WOT + w * 32768 + pi * 1024, stg[8];
  lptr wotp = lds + LDS_W + w * 1024 + (4 * hh + ((lane & 15) >> 2)) * 64 + ((lane >> 4) & 1) * 32 + (lane & 3) * 8;
#pragma unroll
  for (int i = 1; i < 8; ++i) { stg[i] = gbase + w * 4096 + (i - 1 + (i - 1 >= jb)) * SLOT; asm volatile("" : "+s"(stg[i])); }
  PIN(wov); PIN(l0); PIN(fp); PIN(fp2); PIN(wotp); asm volatile("" : "+s"(wos), "+s"(f1s), "+s"(f2s), "+s"(wots), "+s"(po), "+s"(po4));

  bf16x8 xf[28], wk[2], wq[2];
  f32x16 accK = {}, accQ = {};
  const unsigned xr = 4096 * w + r32 * 128 + ((hh ^ ((r32 >> 1) & 7)) * 16);
  const unsigned trl = (8 * hh + ((lane & 15) >> 2)) * 64 + ((lane >> 4) & 1) * 32 + (lane & 3) * 8 + ((lane & 19) == 18 ? 8 : 0);
  lptr ldh = lds + 2 * SLOT, aq = lds + LDS_W + trl; PIN(ldh); PIN(aq);
  const bool gl = (lane & 19) == 19;
  const unsigned kdl = gl ? 8u : 8192u, kwrap = gl ? 8u : (unsigned)(-2 * 8192);
  lptr ak = lds + (gl ? LDS_GW + (trl & ~63u) : LDS_W + trl);
#define KQ_RD(n) do { const int c_ = ((n) >> 2) % 3; xf[n] = ldsr((c_ == 2 ? ldh : lds + c_ * SLOT) + (xr ^ (32 * ((n) & 3)))); \
    if ((n) && !((n) & 3)) { ak += c_ ? kdl : kwrap; PIN(ak); } \
    wk[(n) & 1] = trr2(ak + ((n) & 3) * 1024, 256); wq[(n) & 1] = trr2(aq + c_ * 8192 + 4096 + ((n) & 3) * 1024, 256); } while (0)
#define KQ_STEP(n, MEM) do { SBAR(); if ((n) < 27) KQ_RD((n) + 1); SBAR(); \
    accK = MFMA(wk[(n) & 1], xf[n], accK); accQ = MFMA(wq[(n) & 1], xf[n], accQ); PIN(accK); PIN(accQ); SBAR(); MEM; SBAR(); } while (0)
#define KQ_CHUNK(n, c) do { KQ_STEP(n, DMA_W(c); DMA_X(c, 0)); KQ_STEP((n) + 1, DMA_X(c, 1)); KQ_STEP((n) + 2, DMA_X(c, 2)); KQ_STEP((n) + 3, DMA_X(c, 3)); WAIT_BAR(5); } while (0)
  asm volatile("s_waitcnt vmcnt(7)" : "+v"(gw) :: "memory");
  *(LAS unsigned short*)(lds + LDS_GW + lane * 64 + w * 8) = (unsigned short)gw;
  WAIT_BAR(7); KQ_RD(0);
  KQ_STEP(0, DMA_W(2); DMA_X(2, 0)); KQ_STEP(1, DMA_X(2, 1); DMA_X(2, 2)); KQ_STEP(2, DMA_X(2, 3)); WAIT_BAR(6);
  KQ_CHUNK(3, 3); KQ_CHUNK(7, 4); KQ_CHUNK(11, 5); KQ_CHUNK(15, 6);
  KQ_STEP(19, DMA_V(0)); KQ_STEP(20, DMA_V(1)); KQ_STEP(21, (void)0); KQ_STEP(22, (void)0); WAIT_BAR(2);
  KQ_STEP(23, (void)0); KQ_STEP(24, (void)0); KQ_STEP(25, (void)0); KQ_STEP(26, (void)0); KQ_STEP(27, (void)0);
  bf16x8 k0 = pack8(accK, 0), k1 = pack8(accK, 1);
  { u32x4 t = __builtin_bit_cast(u32x4, k1); t[2] = hh ? 0x00003f80u : t[2]; t[3] = hh ? 0u : t[3]; k1 = __builtin_bit_cast(bf16x8, t); }
  const unsigned kvl = lane * 16, kvo = gbase + jb * SLOT + w * 4096;
  const lptr own = lds + w * 4096 + lane * 16;
  __builtin_amdgcn_raw_buffer_store_b128(__builtin_bit_cast(u32x4, k0), rsWS, kvl, kvo, DEV);
  __builtin_amdgcn_raw_buffer_store_b128(__builtin_bit_cast(u32x4, k1), rsWS, kvl + 1024, kvo, DEV);
  *(LAS bf16x8*)own = k0; *(LAS bf16x8*)(own + 1024) = k1;
  asm volatile("" ::: "memory"); SBAR(); DMA_V(2); DMA_V(3); SBAR();
  float gs;
  { auto r = __builtin_amdgcn_permlane32_swap(__float_as_uint(accK[12]), __float_as_uint(accK[12]), false, false);
    gs = __builtin_amdgcn_rcpf(1.0f + __builtin_amdgcn_exp2f(__uint_as_float(r[1]) * (br ? LOG2E : -LOG2E))); PIN(gs); }
#define FLAG_ST(off, so) do { __builtin_amdgcn_raw_buffer_store_b64(__builtin_bit_cast(u32x2, epoch), rsWS, l0 + (off), so, DEV); asm volatile("" ::: "memory"); } while (0)
#define FLAG1(i) do { if ((i) == 0) { PIN(epoch); epoch += 1; } else { fp += 8; PIN(fp); } FLAG_ST(8 * (i), f1s); } while (0)
  unsigned long long kflag;
#define POLL() asm volatile("buffer_load_dwordx2 %0, %1, %2, 0 offen sc1" : "=v"(kflag) : "v"(fp), "s"(rsWS) : "memory")
#pragma unroll
  for (int i = 0; i < 16; ++i) accQ[i] *= QSCALE;
  bf16x8 q0 = pack8(accQ, 0), q1 = pack8(accQ, 1);
  { u32x4 t = __builtin_bit_cast(u32x4, q1); t[2] = hh ? 0u : t[2]; t[3] = hh ? 0u : t[3]; q1 = __builtin_bit_cast(bf16x8, t); }
  const f32x16 zero = {};
  {
    f32x16 s = MFMA(k0, q0, zero); s = MFMA(k1, q1, s);
    const float m = rowmax16(s);
    u32x4 t = __builtin_bit_cast(u32x4, q1); t[2] = hh ? (cvtpk(-m, 0.f) & 0xffffu) : t[2]; q1 = __builtin_bit_cast(bf16x8, t); }
  PIN(q1);

  const unsigned kvd = lds0 + w * 4096;
#define DMA_P(s, p) dma16_dev<(p) * 1024>(kvd + ((s) & 3) * SLOT, kvl, rsWS, stg[s])
#define DMA_V4(p) dma16_dev<(p) * 1024>(lds0 + LDS_V4 + (w - 1) * 2048, kvl, rsWS, stg[4])
#define DMA_V5(p) dma16_dev<(p) * 1024>(kvd, kvl, rsWS, stg[5])
  bf16x8 wot;
  bf16x8 wo[32];
#define WO_LD(ks) do { wo[ks] = __builtin_bit_cast(bf16x8, __builtin_amdgcn_raw_buffer_load_b128(rsWS, kvl + ((ks) & 3) * 1024, wbase + ((ks) >> 2) * 4096, DEV)); asm volatile("" ::: "memory"); } while (0)

  u32x4 pl[8];
#define PL_LD(i) do { pl[i] = __builtin_amdgcn_raw_buffer_load_b128(rsWS, kvl + 1024 * ((i) & 3), (i) & 4 ? po4 : po, DEV); asm volatile("" ::: "memory"); } while (0)
#define MX4(v) max(max(v[0], v[1]), max(v[2], v[3]))
#define PL_LD2(i) do { PL_LD(i); PL_LD((i) + 1); } while (0)
  f32x16 o = {}, sc[65];
  u32x4 P[16][2]; bf16x8 vf[128], wf[28]; f32x16 accV = {};
  lptr aw = ldh + SLOT + trl; PIN(aw);
  const lptr fb = lds + lane * 16;
  lptr fbh = fb + 2 * SLOT; PIN(fbh);
  lptr fv, fb4; int odd = 0xdddddddd; PIN(odd);
#define TL(n) ((((n) >> 4) & 1 ? fbh : fb) + ((n) & 15) * 4096)
#define EX(i) S[i] = __builtin_amdgcn_exp2f(S[i])
#define CV(i) do { if (!((i) & 3)) asm volatile("" : "=v"(P[N][(i) >> 2])); P[N][(i) >> 2][(i) & 3] = __builtin_amdgcn_perm(__float_as_uint(S[2 * (i) + 1]), __float_as_uint(S[2 * (i)]), 0x07060302u); } while (0)
#define QK0(ld) do { sc[N + 1] = MFMA(k0, q0, zero); if (ld) k0 = ldsr(TL(N + 2)); else PIN(sc[N + 1]); } while (0)
#define QK1(ld) do { sc[N + 1] = MFMA(k1, q1, sc[N + 1]); if (ld) k1 = ldsr(TL(N + 2) + 1024); else PIN(sc[N + 1]); } while (0)
#define PV(n, h) do { o = MFMA(vf[2 * (n) + (h)], __builtin_bit_cast(bf16x8, P[n][h]), o); PIN(o); } while (0)
#define VL(n, h) do { const lptr p_ = fv + (n) * 4096 + 2048 + 1024 * (h);     \
    vf[2 * (n) + (h)] = __builtin_shufflevector(*(const LAS s16x4*)p_, *(const LAS s16x4*)(p_ + 512), 0, 1, 2, 3, 4, 5, 6, 7); } while (0)
#define SPV(v, p, h) o = __builtin_amdgcn_smfmac_f32_32x32x32_bf16(__builtin_bit_cast(bf8, v), __builtin_bit_cast(bf16v, __builtin_shufflevector(p, p, \
    8 * (h), 8 * (h) + 1, 8 * (h) + 2, 8 * (h) + 3, 8 * (h) + 4, 8 * (h) + 5, 8 * (h) + 6, 8 * (h) + 7)), o, odd, 0, 0)
#define PR(n, h) do { SPV(vf[2 * (n) + (h)], sc[n], h); PIN(o); } while (0)
#define VR(n, h) vf[2 * (n) + (h)] = ldsr(((n) >> 3 == 4 ? fb4 + ((n) & 7) * 2048 : TL((n) >> 3 == 5 ? (n) & 7 : (n)) + 2048) + 1024 * (h))
#define VP(j) do { accV = MFMA(xf[j], wf[j], accV); PIN(accV); } while (0)
#define WL(j) wf[j] = trr2(aw + (j) * 1024, 256)
#define WAITV(N) asm volatile("s_waitcnt vmcnt(" #N ")" ::: "memory")
#define VPACK() do { u32x4 a_ = __builtin_bit_cast(u32x4, pack8(accV, 0)), b_ = __builtin_bit_cast(u32x4, pack8(accV, 1)); const unsigned pv_ = r32 == 28 ? 0x3f803f80u : 0u; \
    _Pragma("unroll") for (int i = 0; i < 4; ++i) { a_[i] = r32 >= 28 ? pv_ : a_[i]; b_[i] = r32 >= 28 ? pv_ : b_[i]; } \
    _Pragma("unroll") for (int i = 0; i < 2; ++i) { auto r_ = __builtin_amdgcn_permlane32_swap(a_[i], a_[i + 2], false, false); a_[i] = r_[0]; a_[i + 2] = r_[1]; \
      r_ = __builtin_amdgcn_permlane32_swap(b_[i], b_[i + 2], false, false); b_[i] = r_[0]; b_[i + 2] = r_[1]; } \
    __builtin_amdgcn_raw_buffer_store_b128(a_, rsWS, kvl + 2048, kvo, DEV); __builtin_amdgcn_raw_buffer_store_b128(b_, rsWS, kvl + 3072, kvo, DEV); \
    *(LAS u32x4*)(own + 2048) = a_; *(LAS u32x4*)(own + 3072) = b_; asm volatile("" ::: "memory"); } while (0)
  const u32x4 sent = {~0u, ~0u, ~0u, ~0u};
#define SENT(i) do { __builtin_amdgcn_raw_buffer_store_b128(sent, rsWS, kvl + 1024 * (i), op_own, DEV); asm volatile("" ::: "memory"); } while (0)
#define WOT_ST() do { __builtin_amdgcn_raw_buffer_store_b128(__builtin_bit_cast(u32x4, wot), rsWS, kvl, wots, DEV); asm volatile("" ::: "memory"); } while (0)
#define STG_BAR(N) asm volatile("s_waitcnt vmcnt(" #N ")\n\ts_barrier" ::: "memory")
#define FLAG2() do { FLAG_ST(0, f2s); fp = fp2; } while (0)
#define STEP4(n, PRIO, M0, M1, M2, M3) do { constexpr int N = n; f32x16& S = sc[N]; SBAR(); if ((PRIO) >= 0) SETPRIO((PRIO) & 3); \
    EX(0); EX(1); PIN(S); SBAR(); M0; SBAR(); \
    EX(2); CV(0); EX(3); EX(4); CV(1); EX(5); PIN(S); PIN(P[N][0]); SBAR(); M1; SBAR(); \
    EX(6); CV(2); EX(7); EX(8); CV(3); EX(9); PIN(S); PIN(P[N][0]); SBAR(); M2; SBAR(); \
    EX(10); CV(4); EX(11); EX(12); CV(5); EX(13); PIN(S); PIN(P[N][1]); SBAR(); M3; SBAR(); \
    EX(14); CV(6); EX(15); CV(7); PIN(P[N][1]); SBAR(); } while (0)
#define STEP3(n, PRIO, M0, M1, M2) do { constexpr int N = n; f32x16& S = sc[N]; SBAR(); if ((PRIO) >= 0) SETPRIO((PRIO) & 3); \
    EX(0); EX(1); PIN(S); SBAR(); M0; SBAR(); \
    EX(2); CV(0); EX(3); EX(4); CV(1); EX(5); PIN(S); PIN(P[N][0]); SBAR(); M1; SBAR(); \
    EX(6); CV(2); EX(7); EX(8); CV(3); EX(9); EX(10); CV(4); PIN(S); PIN(P[N][0]); PIN(P[N][1]); SBAR(); M2; SBAR(); \
    EX(11); EX(12); CV(5); EX(13); EX(14); CV(6); EX(15); CV(7); PIN(P[N][1]); SBAR(); } while (0)
#define STEP1R(n, PRIO, M0) do { constexpr int N = n; f32x16& S = sc[N]; SBAR(); if ((PRIO) >= 0) SETPRIO((PRIO) & 3); \
    EX(0); EX(1); EX(2); EX(3); EX(4); EX(5); PIN(S); SBAR(); M0; SBAR(); \
    EX(6); EX(7); EX(8); EX(9); EX(10); EX(11); EX(12); EX(13); EX(14); EX(15); PIN(S); SBAR(); } while (0)
#define STEP2R(n, PRIO, M0, M1) do { constexpr int N = n; f32x16& S = sc[N]; SBAR(); if ((PRIO) >= 0) SETPRIO((PRIO) & 3); \
    EX(0); EX(1); PIN(S); SBAR(); M0; SBAR(); \
    EX(2); EX(3); EX(4); EX(5); PIN(S); SBAR(); M1; SBAR(); \
    EX(6); EX(7); EX(8); EX(9); EX(10); EX(11); EX(12); EX(13); EX(14); EX(15); PIN(S); SBAR(); } while (0)
#define STEP3R(n, PRIO, M0, M1, M2) do { constexpr int N = n; f32x16& S = sc[N]; SBAR(); if ((PRIO) >= 0) SETPRIO((PRIO) & 3); \
    EX(0); EX(1); PIN(S); SBAR(); M0; SBAR(); \
    EX(2); EX(3); EX(4); EX(5); PIN(S); SBAR(); M1; SBAR(); \
    EX(6); EX(7); EX(8); EX(9); EX(10); PIN(S); SBAR(); M2; SBAR(); \
    EX(11); EX(12); EX(13); EX(14); EX(15); PIN(S); SBAR(); } while (0)
#define STEP4R(n, PRIO, M0, M1, M2, M3) do { constexpr int N = n; f32x16& S = sc[N]; SBAR(); if ((PRIO) >= 0) SETPRIO((PRIO) & 3); \
    EX(0); EX(1); EX(2); PIN(S); SBAR(); M0; SBAR(); \
    EX(3); EX(4); EX(5); EX(6); PIN(S); SBAR(); M1; SBAR(); \
    EX(7); EX(8); EX(9); EX(10); PIN(S); SBAR(); M2; SBAR(); \
    EX(11); EX(12); EX(13); EX(14); PIN(S); SBAR(); M3; SBAR(); \
    EX(15); PIN(S); SBAR(); } while (0)
#define STEP5R(n, PRIO, M0, M1, M2, M3, M4) do { constexpr int N = n; f32x16& S = sc[N]; SBAR(); if ((PRIO) >= 0) SETPRIO((PRIO) & 3); \
    EX(0); PIN(S); SBAR(); M0; SBAR(); \
    EX(1); EX(2); EX(3); PIN(S); SBAR(); M1; SBAR(); \
    EX(4); EX(5); EX(6); EX(7); PIN(S); SBAR(); M2; SBAR(); \
    EX(8); EX(9); EX(10); PIN(S); SBAR(); M3; SBAR(); \
    EX(11); EX(12); EX(13); EX(14); PIN(S); SBAR(); M4; SBAR(); \
    EX(15); PIN(S); SBAR(); } while (0)
#define STEP6R(n, PRIO, M0, M1, M2, M3, M4, M5) do { constexpr int N = n; f32x16& S = sc[N]; SBAR(); if ((PRIO) >= 0) SETPRIO((PRIO) & 3); \
    EX(0); PIN(S); SBAR(); M0; SBAR(); \
    EX(1); EX(2); EX(3); PIN(S); SBAR(); M1; SBAR(); \
    EX(4); EX(5); PIN(S); SBAR(); M2; SBAR(); \
    EX(6); EX(7); EX(8); PIN(S); SBAR(); M3; SBAR(); \
    EX(9); EX(10); PIN(S); SBAR(); M4; SBAR(); \
    EX(11); EX(12); EX(13); PIN(S); SBAR(); M5; SBAR(); \
    EX(14); EX(15); PIN(S); SBAR(); } while (0)
#define STEPLR(n, PRIO, M0, M1) do { constexpr int N = n; f32x16& S = sc[N]; SBAR(); if ((PRIO) >= 0) SETPRIO((PRIO) & 3); \
    EX(0); EX(1); EX(2); EX(3); EX(4); EX(5); PIN(S); SBAR(); M0; SBAR(); \
    EX(6); EX(7); EX(8); EX(9); PIN(S); SBAR(); M1; SBAR(); \
    EX(10); EX(11); EX(12); EX(13); EX(14); EX(15); PIN(S); SBAR(); } while (0)
  WAIT_BAR(4);
  k0 = ldsr(TL(0)); k1 = ldsr(TL(0) + 1024); WL(0); WL(1); SBAR(); sc[0] = MFMA(k0, q0, zero); k0 = ldsr(TL(1)); sc[0] = MFMA(k1, q1, sc[0]); k1 = ldsr(TL(1) + 1024);
  STEP6R(0, 2, QK0(1), QK1(1); SBAR(); DMA_WO(), VP(0); WL(2), VP(1); WL(3), VP(2); WL(4), VP(3); WL(5));
  STEP6R(1, -1, QK0(1), QK1(1), VP(4); WL(6), VP(5); WL(7), VP(6); WL(8), VP(7); WL(9));
  WAITV(3); FLAG1(0);
  STEP6R(2, 1, QK0(1), QK1(1), VP(8); WL(10), VP(9); WL(11), VP(10); WL(12), VP(11); WL(13));
  STEP6R(3, -1, QK0(1), QK1(1); SBAR(); POLL(), VP(12); WL(14), VP(13); WL(15), VP(14), VP(15));
  STG_BAR(3);
  WL(16); WL(17);
  STEP6R(4, 0, QK0(1), QK1(1), VP(16); WL(18), VP(17); WL(19), VP(18); WL(20), VP(19); WL(21));
  WAITV(0); await_flags(kflag, fp, rsWS, epoch, 0xffull & ~(1ull << jb)); DMA_P(1, 0); DMA_P(1, 1);
  STEP6R(5, -1, QK0(1), QK1(1); SBAR(); DMA_P(2, 0), VP(20); WL(22), VP(21); WL(23), VP(22); WL(24), VP(23); WL(25); SBAR(); DMA_P(2, 1));
  STEP6R(6, 3, VP(24); WL(26), VP(25); WL(27), VP(26), VP(27), QK0(0), QK1(0));
  VPACK();
  STEP1R(7, -1, (void)0);
  WAIT_BAR(4);
  k0 = ldsr(TL(8)); k1 = ldsr(TL(8) + 1024); VR(0, 0); VR(0, 1); SBAR(); sc[8] = MFMA(k0, q0, zero); k0 = ldsr(TL(9)); sc[8] = MFMA(k1, q1, sc[8]); k1 = ldsr(TL(9) + 1024);
  STEP3(8, 2, QK0(1), QK1(1), PR(0, 0); VR(1, 0));
  STEP3(9, -1, QK0(1), QK1(1), PR(0, 1); VR(1, 1));
  WAITV(0); FLAG1(1);
  STEP4(10, 1, QK0(1), QK1(1); SBAR(); DMA_P(3, 0), PR(1, 0); VR(2, 0), PR(1, 1); VR(2, 1));
  STEP3(11, -1, QK0(1), QK1(1); SBAR(); DMA_P(3, 1), PR(2, 0); VR(3, 0));
  STEP3(12, 0, QK0(1), QK1(1), PR(2, 1); VR(3, 1));
  STEP3(13, -1, QK0(1), QK1(1); SBAR(); POLL(), PR(3, 0); VR(4, 0));
  STG_BAR(6);
  STEP4(14, 3, QK0(1), QK1(1), PR(3, 1); VR(4, 1), PR(4, 0); VR(5, 0));
  WAITV(0); await_flags(kflag, fp, rsWS, epoch, 0xffull & ~(1ull << jb)); DMA_P(1, 2); DMA_P(1, 3);
  STEP4(15, -1, QK0(1), QK1(1); SBAR(); DMA_P(2, 2), PR(4, 1); VR(5, 1), PR(5, 0); VR(6, 0); SBAR(); DMA_P(2, 3));
  STEP4R(16, 2, QK0(1), QK1(1); SBAR(); DMA_P(3, 2), PR(5, 1); VR(6, 1), PR(6, 0); VR(7, 0); SBAR(); DMA_P(3, 3));
  STEP3R(17, -1, QK0(1), QK1(1); SBAR(); DMA_P(4, 0), PR(6, 1); VR(7, 1); SBAR(); DMA_P(4, 1));
  STEP3R(18, 1, QK0(1), QK1(1); SBAR(); DMA_V4(2), PR(7, 0); SBAR(); DMA_V4(3));
  wot = trr2(wotp, 512);
  STEP3R(19, -1, QK0(1), QK1(1); SBAR(); SENT(0), PR(7, 1); SBAR(); SENT(1));
  STEP2R(20, 0, QK0(1), QK1(1); SBAR(); WOT_ST());
  STEP2R(21, -1, QK0(1), QK1(1));
  STG_BAR(7);
  VR(16, 0); VR(16, 1);
  STEP5R(22, 3, QK0(1), QK1(1); SBAR(); DMA_V5(2), PR(16, 0); VR(22, 0), PR(16, 1); VR(22, 1), PR(22, 0); VR(17, 0));
  STEP5R(23, -1, QK0(1), QK1(1); SBAR(); DMA_V5(3), PR(22, 1); VR(23, 0), PR(17, 0); VR(23, 1), PR(23, 0); VR(17, 1));
  STEP5R(24, 2, QK0(1), QK1(1), PR(23, 1); VR(24, 0), PR(17, 1); VR(24, 1), PR(24, 0); VR(18, 0));
  STEP5R(25, -1, QK0(1), QK1(1); SBAR(); DMA_P(5, 0), PR(24, 1); VR(25, 0), PR(18, 0); VR(25, 1), PR(25, 0); VR(18, 1));
  STEP5R(26, 1, QK0(1), QK1(1), PR(25, 1); VR(26, 0), PR(18, 1); VR(26, 1), PR(26, 0); VR(19, 0));
  STEP5R(27, -1, QK0(1), QK1(1); SBAR(); DMA_P(5, 1), PR(26, 1); VR(27, 0), PR(19, 0); VR(27, 1), PR(27, 0); VR(19, 1));
  STEP5R(28, 0, QK0(1), QK1(1), PR(27, 1); VR(28, 0), PR(19, 1); VR(28, 1), PR(28, 0); VR(20, 0));
  STEP5R(29, -1, QK0(1), QK1(1), PR(28, 1); VR(29, 0), PR(20, 0); VR(29, 1), PR(29, 0); VR(20, 1));
  STG_BAR(7);
  fb4 = fb + LDS_V4; fv = lds + r32 * 16 + hh * 8; PIN(fb4); PIN(fv);
  STEP5R(30, 3, QK0(1), QK1(1), PR(29, 1); VR(30, 0), PR(20, 1); VR(30, 1), PR(30, 0); VR(21, 0));
  STEP5R(31, -1, QK0(1), QK1(1), PR(30, 1); VR(31, 0), PR(21, 0); VR(31, 1), PR(31, 0); VR(21, 1));
  STEP5R(32, 2, QK0(1), QK1(1), PR(31, 1); VR(32, 0), PR(21, 1); VR(32, 1), PR(32, 0); VL(8, 0));
  WAITV(4); FLAG2();
  STEP5R(33, -1, QK0(1), QK1(1); SBAR(); DMA_P(6, 0), PR(32, 1); VR(33, 0), PV(8, 0); VR(33, 1), PR(33, 0); VL(8, 1));
  STEP5R(34, 1, QK0(1), QK1(1), PR(33, 1); VR(34, 0), PV(8, 1); VR(34, 1), PR(34, 0); VL(9, 0));
  STEP5R(35, -1, QK0(1), QK1(1); SBAR(); DMA_P(6, 1), PR(34, 1); VR(35, 0), PV(9, 0); VR(35, 1), PR(35, 0); VL(9, 1));
  STEP5R(36, 0, QK0(1), QK1(1), PR(35, 1); VR(36, 0), PV(9, 1); VR(36, 1), PR(36, 0); VL(10, 0));
  STEP5R(37, -1, QK0(1), QK1(1), PR(36, 1); VR(37, 0), PV(10, 0); VR(37, 1), PR(37, 0); VL(10, 1));
  STG_BAR(3);
  STEP5R(38, 3, QK0(1); SBAR(); DMA_P(6, 2), QK1(1); SBAR(); DMA_P(7, 2), PR(37, 1); VR(38, 0), PV(10, 1); VR(38, 1), PR(38, 0); VL(11, 0));
  STEP5R(39, -1, QK0(1); SBAR(); DMA_P(6, 3), QK1(1); SBAR(); DMA_P(7, 3), PR(38, 1); VR(39, 0), PV(11, 0); VR(39, 1), PR(39, 0); VL(11, 1); SBAR(); POLL());
  STEP5R(40, 2, QK0(1), QK1(1), PR(39, 1); VR(40, 0), PV(11, 1); VR(40, 1), PR(40, 0); VL(12, 0));
  STEP5R(41, -1, QK0(1); SBAR(); DMA_P(7, 0), QK1(1), PR(40, 1); VR(41, 0), PV(12, 0); VR(41, 1), PR(41, 0); VL(12, 1));
  WAITV(1); await_flags(kflag, fp, rsWS, epoch, ~0ull);
  STEP5R(42, 1, QK0(1), QK1(1); SBAR(); WO_LD(0), PR(41, 1); VR(42, 0), PV(12, 1); VR(42, 1), PR(42, 0); VL(13, 0); SBAR(); WO_LD(1));
  STEP5R(43, -1, QK0(1), QK1(1); SBAR(); DMA_P(7, 1); SBAR(); WO_LD(2), PR(42, 1); VR(43, 0), PV(13, 0); VR(43, 1), PR(43, 0); VL(13, 1); SBAR(); WO_LD(3));
  STEP5R(44, 0, QK0(1), QK1(1), PR(43, 1); VR(44, 0), PV(13, 1); VR(44, 1), PR(44, 0); VL(14, 0); SBAR(); WO_LD(4));
  STEP5R(45, -1, QK0(1), QK1(1); SBAR(); WO_LD(5), PR(44, 1); VR(45, 0), PV(14, 0); VR(45, 1), PR(45, 0); VL(14, 1); SBAR(); WO_LD(6));
  STG_BAR(11);
  STEP5R(46, 3, QK0(1), QK1(1), PR(45, 1); VR(46, 0), PV(14, 1); VR(46, 1), PR(46, 0); VL(15, 0); SBAR(); WO_LD(7));
  STEP5R(47, -1, QK0(1), QK1(1); SBAR(); WO_LD(8), PR(46, 1); VR(47, 0), PV(15, 0); VR(47, 1), PR(47, 0); VL(15, 1); SBAR(); WO_LD(9));
  STEP5R(48, 2, QK0(1), QK1(1), PR(47, 1); VR(48, 0), PV(15, 1); VR(48, 1), PR(48, 0); VR(49, 0); SBAR(); WO_LD(10));
  STEP4R(49, -1, QK0(1), QK1(1); SBAR(); WO_LD(11), PR(48, 1); VR(49, 1), PR(49, 0); VR(50, 0); SBAR(); WO_LD(12));
  STEP4R(50, 1, QK0(1), QK1(1), PR(49, 1); VR(50, 1), PR(50, 0); VR(51, 0); SBAR(); WO_LD(13));
  STEP4R(51, -1, QK0(1), QK1(1); SBAR(); WO_LD(14), PR(50, 1); VR(51, 1), PR(51, 0); VR(52, 0); SBAR(); WO_LD(15));
  STEP4R(52, 0, QK0(1), QK1(1), PR(51, 1); VR(52, 1), PR(52, 0); VR(53, 0); SBAR(); WO_LD(16));
  STEP4R(53, -1, QK0(1), QK1(1); SBAR(); WO_LD(17), PR(52, 1); VR(53, 1), PR(53, 0); VR(54, 0); SBAR(); WO_LD(18));
  STG_BAR(17);
  STEP4R(54, 3, QK0(1), QK1(1), PR(53, 1); VR(54, 1), PR(54, 0); VR(55, 0); SBAR(); WO_LD(19));
  STEP4R(55, -1, QK0(1), QK1(1); SBAR(); WO_LD(20), PR(54, 1); VR(55, 1), PR(55, 0); VR(56, 0); SBAR(); WO_LD(21));
  STEP4R(56, 2, QK0(1), QK1(1), PR(55, 1); VR(56, 1), PR(56, 0); VR(57, 0); SBAR(); WO_LD(22));
  STEP4R(57, -1, QK0(1), QK1(1); SBAR(); WO_LD(23), PR(56, 1); VR(57, 1), PR(57, 0); VR(58, 0); SBAR(); WO_LD(24));
  STEP4R(58, 1, QK0(1), QK1(1), PR(57, 1); VR(58, 1), PR(58, 0); VR(59, 0); SBAR(); WO_LD(25));
  STEP4R(59, -1, QK0(1), QK1(1); SBAR(); WO_LD(26), PR(58, 1); VR(59, 1), PR(59, 0); VR(60, 0); SBAR(); WO_LD(27));
  STEP4R(60, 0, QK0(1), QK1(1), PR(59, 1); VR(60, 1), PR(60, 0); VR(61, 0); SBAR(); WO_LD(28));
  STEP4R(61, -1, QK0(1), QK1(1); SBAR(); WO_LD(29), PR(60, 1); VR(61, 1), PR(61, 0); VR(62, 0); SBAR(); WO_LD(30));
  STEP4R(62, 3, QK0(0), QK1(0), PR(61, 1); VR(62, 1), PR(62, 0); VR(63, 0); SBAR(); WO_LD(31));
  STEPLR(63, -1, PR(62, 1); VR(63, 1), PR(63, 0));
  SETPRIO(0);
  PR(63, 1);

  u32x4 oa, ob; float l; f32x2 t;
#define O2(i) (f32x2){o[2 * (i)], o[2 * (i) + 1]}
#define NORM() do { auto r = __builtin_amdgcn_permlane32_swap(__float_as_uint(o[12]), __float_as_uint(o[12]), false, false); l = __uint_as_float(r[1]); \
    const float inv = gs * __builtin_amdgcn_rcpf(l); \
    t = ((O2(0) + O2(1)) + (O2(2) + O2(3))) + ((O2(4) + O2(5)) + (O2(6) + O2(7))); \
    _Pragma("unroll") for (int i = 0; i < 16; ++i) o[i] *= inv; \
    oa = __builtin_bit_cast(u32x4, pack8(o, 0)); ob = __builtin_bit_cast(u32x4, pack8(o, 1)); ob[2] = hh ? 0u : ob[2]; ob[3] = hh ? 0u : ob[3]; } while (0)
#define O_ST() do { __builtin_amdgcn_raw_buffer_store_b128(oa, rsWS, kvl, op_own, DEV); __builtin_amdgcn_raw_buffer_store_b128(ob, rsWS, kvl + 1024, op_own, DEV); } while (0)
  NORM(); PIN(oa); PIN(ob);
  {
    const bool bad = !(fabsf(t[0] + t[1] + gs) < INFINITY) || !(l < 1e30f) || !(l > 1e-30f);
    if (__builtin_expect(__any(bad), 0)) {
      float m = -INFINITY; o = zero;
      u32x4 qz = __builtin_bit_cast(u32x4, q1); qz[2] = hh ? 0u : qz[2]; const bf16x8 q1z = __builtin_bit_cast(bf16x8, qz);
      for (int kt = 0; kt < 64; ++kt) {
        const unsigned ko = gbase + kt * 4096;
        const bf16x8 a0 = __builtin_bit_cast(bf16x8, __builtin_amdgcn_raw_buffer_load_b128(rsWS, kvl, ko, DEV));
        const bf16x8 a1 = __builtin_bit_cast(bf16x8, __builtin_amdgcn_raw_buffer_load_b128(rsWS, kvl + 1024, ko, DEV));
        const bf16x8 b0 = __builtin_bit_cast(bf16x8, __builtin_amdgcn_raw_buffer_load_b128(rsWS, kvl + 2048, ko, DEV));
        const bf16x8 b1 = __builtin_bit_cast(bf16x8, __builtin_amdgcn_raw_buffer_load_b128(rsWS, kvl + 3072, ko, DEV));
        f32x16 s = MFMA(a0, q0, zero); s = MFMA(a1, q1z, s);
        const float mn = fmaxf(m, rowmax16(s)), al = __builtin_amdgcn_exp2f(m - mn); m = mn;
#pragma unroll
        for (int i = 0; i < 16; ++i) { o[i] *= al; s[i] = __builtin_amdgcn_exp2f(s[i] - mn); }
        SPV(b0, s, 0); SPV(b1, s, 1);
      }
      NORM();
#pragma unroll
      for (int i = 0; i < 4; ++i) { oa[i] = min(oa[i], 0xfffffffeu); ob[i] = min(ob[i], 0xfffffffeu); }
      O_ST(); asm volatile("" ::: "memory");
    } else O_ST(); }
  __builtin_amdgcn_s_barrier();

  f32x16 y[4] = {};
  const lptr rb = lds + w * 14336 + hh * 7168 + r32 * 224;
  asm volatile("" ::: "memory");
  {
    PL_LD2(0); PL_LD2(2);
    PL_LD2(4); PL_LD2(6);
#define MISS2(i) __any(max(MX4(pl[i]), MX4(pl[(i) + 1])) == ~0u)
#pragma unroll
    for (int g = 0; g < 4; ++g) {
      if (MISS2(2 * g)) {
        unsigned spins = 0;
#pragma unroll 1
        do { __builtin_amdgcn_s_sleep(4); asm volatile("" ::: "memory");
#pragma unroll
          for (int i = 2 * g; i < 8; ++i) PL_LD(i);
        } while (MISS2(2 * g) && ++spins < SPIN_CAP);
      }
      if (g < 3) {
#pragma unroll
        for (int i = 2 * g; i < 2 * g + 2; ++i)
#pragma unroll
          for (int nt = 0; nt < 4; ++nt) y[nt] = MFMA(wo[4 * i + nt], __builtin_bit_cast(bf16x8, pl[i]), y[nt]);
        PIN(y[0]); PIN(y[1]); PIN(y[2]); PIN(y[3]); SBAR();
      } else {
#pragma unroll
        for (int nt = 0; nt < 6; ++nt) {
          SBAR();
          if (nt < 4)
#pragma unroll
            for (int i = 6; i < 8; ++i) y[nt] = MFMA(wo[4 * i + nt], __builtin_bit_cast(bf16x8, pl[i]), y[nt]);
          SBAR();
          if (nt > 1)
#pragma unroll
            for (int u = 4 * nt - 8; u < min(4 * nt - 4, 14); ++u)
              *(LAS f32x4*)(rb + u * 16) = (f32x4){y[u >> 2][4 * (u & 3)], y[u >> 2][4 * (u & 3) + 1], y[u >> 2][4 * (u & 3) + 2], y[u >> 2][4 * (u & 3) + 3]};
        }
      }
    } }
  { const int tk = tid / 14;
    unsigned oo = tk * (XROW - 224) + 16 * tid;
    lptr ra = lds + tid * 16, rh = ra + 4 * 14336; PIN(oo); PIN(ra); PIN(rh);
    asm volatile("s_waitcnt lgkmcnt(0)\n\ts_barrier" ::: "memory");
    if (w != 7) {
      f32x4 t[16]; u32x4 r;
#pragma unroll
      for (int k = 0; k < 16; ++k) t[k] = *(const LAS f32x4*)((k & 4 ? rh : ra) + (k & 3) * 14336 + (k >> 3) * 7168);
      SBAR();
#pragma unroll
      for (int e = 0; e < 2; ++e) { const f32x4* q = t + 8 * e;
        const f32x4 m = ((q[0] + q[1]) + (q[2] + q[3])) + ((q[4] + q[5]) + (q[6] + q[7]));
        r[2 * e] = cvtpk(m[0], m[1]); r[2 * e + 1] = cvtpk(m[2], m[3]); }
      __builtin_amdgcn_raw_buffer_store_b128(r, __builtin_amdgcn_make_buffer_rsrc((void*)out, 0, SEQ * XROW, 0x00020000), oo, 32 * ot * XROW + 224 * cq, DEV);
    } else if (bid == 0 && lane == 0) __hip_atomic_store((gu64*)ws, epoch, RLX_AGENT);
  }
}

extern "C" void kernel_launch(void* const* d_in, const int* in_sizes, int n_in, void* d_out, int out_size, void* d_ws, size_t ws_size,
                              hipStream_t stream) {
  gated_mha_fused<<<256, 512, 0, stream>>>((const char*)d_in[0], (const char*)d_in[1], (const char*)d_in[2], (const char*)d_in[4],
                                           (const char*)d_in[5], (const char*)d_in[6], (char*)d_out, (char*)d_ws);
}
```
